```python
import torch

def _softmax(x, axis=-1):
    return torch.softmax(x, dim=axis)
def _gelu(x):
    return torch.nn.functional.gelu(x, approximate="tanh")
def _silu(x):
    return torch.nn.functional.silu(x)
def _sigmoid(x):
    return torch.sigmoid(x)
def _layernorm(x, axis=-1, eps=1e-5):
    m = x.mean(dim=axis, keepdim=True)
    v = x.var(dim=axis, keepdim=True, unbiased=False)
    return (x - m) / torch.sqrt(v + eps)
def _scatter_add(tgt, idx, src, axis=0):
    return tgt.clone().index_add_(axis, idx, src)
def _segment_sum(src, seg, n):
    out = torch.zeros((n,) + src.shape[1:], dtype=src.dtype)
    return out.index_add_(0, seg, src)
def _pad(x, pw):
    flat = tuple(v for pair in reversed(pw) for v in pair)
    return torch.nn.functional.pad(x, flat)

kernel_name = 'gated_mix__sliding_block_cached__glu_gated_bilinear__decode'

def setup_inputs():

    _w = (lambda a: (lambda t: t.to(torch.bfloat16) if t.is_floating_point() else t)(torch.as_tensor(a)))
    def _A_setup():
        B, Sq, D = 2, 1, 512
        Hq, Hkv, Sk, Dh = 32, 32, 4096, 128
        assert Hq % Hkv == 0, (Hq, Hkv)
        return {
            "x": _w((lambda *s: torch.randn(*s, dtype=torch.bfloat16) * 0.05)(B, Sq, D)),
            "k_cache": _w((lambda *s: torch.randn(*s, dtype=torch.bfloat16) * 6.4)(B, Hkv, Sk, Dh)),
            "v_cache": _w((lambda *s: torch.randn(*s, dtype=torch.bfloat16) * 0.05)(B, Hkv, Sk, Dh)),
            "wq": _w((lambda *s: torch.randn(*s, dtype=torch.bfloat16) * 6.4)(D, Hq * Dh) * (D ** -0.5)),
            "wo": _w((lambda *s: torch.randn(*s, dtype=torch.bfloat16) * 0.05)(Hq * Dh, D) * ((Hq * Dh) ** -0.5)),
        }
    _A_in = _A_setup()

    def _B_setup():
        B, S, D, F = 2, 1, 512, 1376
        return {
            "x": _w((lambda *s: torch.randn(*s, dtype=torch.bfloat16) * 0.05)(B, S, D)),
            "w_gate": _w((lambda *s: torch.randn(*s, dtype=torch.bfloat16) * 0.4)(D, F) * (D ** -0.5)),
            "w_up": _w((lambda *s: torch.randn(*s, dtype=torch.bfloat16) * 0.4)(D, F) * (D ** -0.5)),
            "w_down": _w((lambda *s: torch.randn(*s, dtype=torch.bfloat16) * 0.4)(F, D) * (F ** -0.5)),
        }
    _B_in = _B_setup()

    return {
        "A_x": _A_in["x"],
        "A_k_cache": _A_in["k_cache"],
        "A_v_cache": _A_in["v_cache"],
        "A_wq": _A_in["wq"],
        "A_wo": _A_in["wo"],
        "B_x": _B_in["x"],
        "B_w_gate": _B_in["w_gate"],
        "B_w_up": _B_in["w_up"],
        "B_w_down": _B_in["w_down"],
        "gate_w": _w((lambda *s: torch.randn(*s, dtype=torch.bfloat16) * 0.05)(512) * 16.0),
    }

def reference(A_x, A_k_cache, A_v_cache, A_wq, A_wo, B_x, B_w_gate, B_w_up, B_w_down, gate_w):
    def _A_ref(x, k_cache, v_cache, wq, wo):
        B, Sq, D = x.shape
        Hq, Dh = 32, 128
        Hkv, Sk = k_cache.shape[1], k_cache.shape[2]
        W = min(4096, max(256, Sk // 4))
        rep = Hq // Hkv
        q = (lambda x, *d: x.permute(*d))((x @ wq).reshape(B, Sq, Hq, Dh), 0, 2, 1, 3)
        k = (lambda x, n, axis: torch.repeat_interleave(x, n, dim=axis))(k_cache[:, :, Sk - W:, :], rep, axis=1)
        v = (lambda x, n, axis: torch.repeat_interleave(x, n, dim=axis))(v_cache[:, :, Sk - W:, :], rep, axis=1)
        scores = torch.einsum("bhqd,bhkd->bhqk", q, k) * (Dh ** -0.5)
        p = _softmax(scores, axis=-1)
        o = torch.einsum("bhqk,bhkd->bhqd", p, v)
        return (lambda x, *d: x.permute(*d))(o, 0, 2, 1, 3).reshape(B, Sq, Hq * Dh) @ wo

    def _B_ref(x, w_gate, w_up, w_down):
        g = torch.einsum("bsd,df->bsf", x, w_gate)
        u = torch.einsum("bsd,df->bsf", x, w_up)
        h = g * u
        return torch.einsum("bsf,fd->bsd", h, w_down)

    x = A_x
    g = _sigmoid((x * gate_w).sum(-1, keepdims=True))
    a = _A_ref(x, A_k_cache, A_v_cache, A_wq, A_wo)
    b = _B_ref(x, B_w_gate, B_w_up, B_w_down)
    return g * a + (1 - g) * b
```

```cpp
#include <hip/hip_runtime.h>
#include <hip/hip_bf16.h>
#include <stdint.h>

#define LAS __attribute__((address_space(3)))
typedef unsigned u32;
typedef unsigned long long u64;
typedef unsigned u32x4 __attribute__((ext_vector_type(4)));
typedef unsigned u32x2 __attribute__((ext_vector_type(2)));
typedef float f32x4 __attribute__((ext_vector_type(4)));
typedef float f32x2 __attribute__((ext_vector_type(2)));
typedef short s16x4 __attribute__((ext_vector_type(4)));
typedef short s16x8 __attribute__((ext_vector_type(8)));
typedef __bf16 bf16x8 __attribute__((ext_vector_type(8)));
typedef __bf16 bf16x2 __attribute__((ext_vector_type(2)));

#define KV_NT " nt"
#define W_NT " nt"

constexpr u32 WS_EPOCH = 0;
constexpr u32 WS_SQ = 4096;
constexpr u32 WS_SG = WS_SQ + 32 * 8 * 256 * 8;
constexpr u32 WS_SO = WS_SG + 86 * 4 * 128 * 8;
constexpr u32 WS_SF = WS_SO + 32 * 8 * 640;
constexpr u32 WS_SB = WS_SF + 32 * 1024 * 8;
constexpr u32 WS_END = WS_SB + 43 * 1024 * 8;
constexpr u32 WS_TS = 4u << 20;

constexpr u32 L_K = 0, L_WO = 0, L_WD = 16384;
constexpr u32 L_V = 65536, L_WQ = L_V + 32768, L_GU = L_V + 49152;
constexpr u32 L_XS = 131072;
constexpr u32 L_GW = L_XS + 2048;
constexpr u32 L_EP = L_GW + 1024;
constexpr u32 L_ST = L_EP + 256;
constexpr u32 L_QW = L_ST + 4096;
constexpr u32 L_PM = L_QW + 2048;
constexpr u32 L_SM = L_PM + 8 * 528;
constexpr u32 L_OS = L_SM + 64;
constexpr u32 L_END = L_OS + 8 * 272;
static_assert(L_END <= 163840, "LDS");

struct Params {
  const char* x; const char* kc; const char* vc; const char* wq; const char* wo;
  const char* wg; const char* wu; const char* wd; const char* gw; char* out; char* ws;
};

__device__ __forceinline__ void dma16(u32 voff, const char* sbase, u32 lds) {
  asm volatile("s_mov_b32 m0, %2\n\ts_nop 0\n\tglobal_load_lds_dwordx4 %0, %1" W_NT :: "v"(voff), "s"(sbase), "s"(lds) : "memory");
}
__device__ __forceinline__ void dma16kv(u32 voff, const char* sbase, u32 lds) {
  asm volatile("s_mov_b32 m0, %2\n\ts_nop 0\n\tglobal_load_lds_dwordx4 %0, %1" KV_NT :: "v"(voff), "s"(sbase), "s"(lds) : "memory");
}
__device__ __forceinline__ void dma16p(u32 voff, const char* sbase, u32 lds) {
  asm volatile("s_mov_b32 m0, %2\n\ts_nop 0\n\tglobal_load_lds_dwordx4 %0, %1" :: "v"(voff), "s"(sbase), "s"(lds) : "memory");
}
__device__ __forceinline__ void dma4p(u32 voff, const char* sbase, u32 lds) {
  asm volatile("s_mov_b32 m0, %2\n\ts_nop 0\n\tglobal_load_lds_dword %0, %1" :: "v"(voff), "s"(sbase), "s"(lds) : "memory");
}
__device__ __forceinline__ void ld16s(u32x4& d, u32 voff, const char* sbase) {
  asm volatile("global_load_dwordx4 %0, %1, %2 sc1" : "=v"(d) : "v"(voff), "s"(sbase) : "memory");
}
__device__ __forceinline__ void ld8s(u32x2& d, u32 voff, const char* sbase) {
  asm volatile("global_load_dwordx2 %0, %1, %2 sc1" : "=v"(d) : "v"(voff), "s"(sbase) : "memory");
}
__device__ __forceinline__ void st8s(u32 voff, u32x2 d, char* sbase) {
  asm volatile("global_store_dwordx2 %0, %1, %2 sc1" :: "v"(voff), "v"(d), "s"(sbase) : "memory");
}
#define WAITVM(n) asm volatile("s_waitcnt vmcnt(" #n ")" ::: "memory")
#define BARRIER() asm volatile("s_waitcnt lgkmcnt(0)\n\ts_barrier" ::: "memory")
#define PIN(x) asm volatile("" : "+v"(x))

template <typename T> __device__ __forceinline__ T lds_ld(const LAS char* base, u32 off) { return *(const LAS T*)(base + off); }
template <typename T> __device__ __forceinline__ void lds_st(LAS char* base, u32 off, T v) { *(LAS T*)(base + off) = v; }
__device__ __forceinline__ s16x4 trd(const LAS char* base, u32 off) {
  return __builtin_bit_cast(s16x4, __builtin_amdgcn_ds_read_tr16_b64_v4i16((LAS s16x4*)(base + off)));
}
__device__ __forceinline__ bf16x8 cat8(s16x4 a, s16x4 b) {
  s16x8 r = __builtin_shufflevector(a, b, 0, 1, 2, 3, 4, 5, 6, 7); return __builtin_bit_cast(bf16x8, r);
}
__device__ __forceinline__ f32x4 mfma(bf16x8 a, bf16x8 b, f32x4 c) { return __builtin_amdgcn_mfma_f32_16x16x32_bf16(a, b, c, 0, 0, 0); }
__device__ __forceinline__ u32 pkbf(float lo, float hi) {
  f32x2 v = {lo, hi}; return __builtin_bit_cast(u32, __builtin_convertvector(v, bf16x2));
}
__device__ __forceinline__ float rbf(float v) { return __uint_as_float(pkbf(v, v) << 16); }
template <int C> __device__ __forceinline__ float dpp(float v) {
  return __builtin_bit_cast(float, __builtin_amdgcn_mov_dpp(__builtin_bit_cast(int, v), C, 0xf, 0xf, true));
}
constexpr int ROR1 = 0x121, ROR2 = 0x122, ROR4 = 0x124, ROR8 = 0x128, QX1 = 0xB1, QX2 = 0x4E;
__device__ __forceinline__ float sum16(float v) { auto s = __builtin_amdgcn_permlane16_swap(__float_as_uint(v), __float_as_uint(v), false, false); return __uint_as_float(s[0]) + __uint_as_float(s[1]); }
__device__ __forceinline__ float sum32(float v) { auto s = __builtin_amdgcn_permlane32_swap(__float_as_uint(v), __float_as_uint(v), false, false); return __uint_as_float(s[0]) + __uint_as_float(s[1]); }
__device__ __forceinline__ float max32(float v) { auto s = __builtin_amdgcn_permlane32_swap(__float_as_uint(v), __float_as_uint(v), false, false); return fmaxf(__uint_as_float(s[0]), __uint_as_float(s[1])); }
__device__ __forceinline__ float ex2(float v) { return __builtin_amdgcn_exp2f(v); }
constexpr float LOG2E = 1.4426950408889634f;
constexpr u32 SPIN_CAP = 1u << 16;

#define TS(i) do {} while (0)

__global__ void __launch_bounds__(512, 2) fused_decode(Params p) {
  __shared__ __attribute__((aligned(1024))) char smem_[L_END];
  LAS char* sm = (LAS char*)smem_;
  const u32 tid = threadIdx.x, lane = tid & 63;
  const u32 w = __builtin_amdgcn_readfirstlane(tid >> 6);
  const u32 bid = blockIdx.x;
  const u32 h = bid >> 3, sub = bid & 7, b = sub >> 2, s = sub & 3;
  const bool roleG = bid < 172, roleD = bid >= 170;
  const u32 g = lane >> 4, col = lane & 15, r4 = (lane >> 2) & 3, c4 = lane & 3;
  TS(0);
  if (w == 0) dma16p(lane * 16, p.x, L_XS);
  else if (w == 1) dma16p(lane * 16, p.x + 1024, L_XS + 1024);
  else if (w == 2) dma16p(lane * 16, p.gw, L_GW);
  else if (w == 3) dma4p(0u * lane, p.ws + WS_EPOCH, L_EP);
  {
    const u32 f = g | ((w & 1) << 2);
    const u32 vo = g * 8192 + ((((lane & 15) >> 1) ^ f) << 5) + ((lane & 1) << 4);
    const char* base = p.wq + (size_t)(64 * sub + 8 * w) * 8192 + h * 256;
    dma16(vo, base, L_WQ + (8 * w) * 256);
    dma16(vo, base + 4 * 8192, L_WQ + (8 * w + 4) * 256);
  }
  const u32 vo128 = ((((lane & 7) >> 1) ^ ((lane >> 4) & 1)) << 5) + ((lane & 1) << 4);
  if (roleG) {
    const u32 ell = bid >> 2, rg = bid & 3;
    const u32 vo = (lane >> 3) * 5504 + (vo128 ^ ((w & 1) << 6));
    const size_t o = (size_t)(64 * rg + 8 * w) * 5504 + ell * 128;
    dma16(vo, p.wg + o, L_GU + (8 * w) * 128);
    dma16(vo, p.wu + o, L_GU + 8192 + (8 * w) * 128);
  }
  const size_t kvrow = ((size_t)(b * 32 + h) * 4096 + 3072 + 256 * s) * 256;
  const u32 vok = g * 256 + ((col ^ g) << 4);
  const char* kbase = p.kc + kvrow + (size_t)w * 8192;
#pragma unroll
  for (int i = 0; i < 4; ++i) dma16kv(vok ^ (i << 6), kbase + i * 1024, L_K + w * 8192 + i * 1024);
  lds_st<u32x2>(sm, L_PM + w * 528 + lane * 8, u32x2{0u, 0u});
  TS(1);
  WAITVM(4);
  BARRIER();
  TS(2);
  const u32 T32 = __builtin_amdgcn_readfirstlane(lds_ld<u32>(sm, L_EP)) + 1u;
  const u32 T8 = (T32 ^ 0x5Au) & 0xFFu;
  {
    const u32 xa = L_XS + (col & 1) * 1024 + (64 * sub + 8 * g) * 2;
    const u32 tb = L_WQ + (8 * g + r4) * 256 + ((w ^ (r4 | ((g & 1) << 2))) << 5) + c4 * 8;
    f32x4 acc = {0.f, 0.f, 0.f, 0.f};
#pragma unroll
    for (int t = 0; t < 2; ++t) {
      const bf16x8 a = lds_ld<bf16x8>(sm, xa + t * 64);
      const bf16x8 bb = cat8(trd(sm, tb + t * 8192), trd(sm, tb + t * 8192 + 1024));
      acc = mfma(a, bb, acc);
    }
    const float v = (g & 1) ? acc[1] : acc[0];
    if (g < 2) {
      u32x2 d = {(__float_as_uint(v) & 0xFFFFFF00u) | T8, T32};
      st8s((g * 128 + 16 * w + col) * 8, d, p.ws + WS_SQ + (size_t)(h * 8 + sub) * 2048);
    }
  }
  if (roleG) {
    const u32 ell = bid >> 2, rg = bid & 3;
    const u32 hl = 2 * ell + ((w & 3) >> 1);
    const u32 sel = hl >= 43 ? 0x07060302u : 0x05040100u;
    const u32 xa = L_XS + (col & 1) * 1024 + (128 * rg + 16 * g) * 2;
    const u32 tb = L_GU + (w >> 2) * 8192 + (8 * g + r4) * 128 + (((w & 3) ^ ((r4 >> 1) | ((g & 1) << 1))) << 5) + c4 * 8;
    f32x4 acc = {0.f, 0.f, 0.f, 0.f};
#pragma unroll
    for (int t = 0; t < 2; ++t) {
      const u32x4 x0 = lds_ld<u32x4>(sm, xa + t * 128), x1 = lds_ld<u32x4>(sm, xa + t * 128 + 16);
      const u32x4 a = {__builtin_amdgcn_perm(x0[1], x0[0], sel), __builtin_amdgcn_perm(x0[3], x0[2], sel),
                       __builtin_amdgcn_perm(x1[1], x1[0], sel), __builtin_amdgcn_perm(x1[3], x1[2], sel)};
      const bf16x8 bb = cat8(trd(sm, tb + t * 4096), trd(sm, tb + t * 4096 + 512));
      acc = mfma(__builtin_bit_cast(bf16x8, a), bb, acc);
    }
    const float v = (g & 1) ? acc[1] : acc[0];
    if (g < 2) {
      u32x2 d = {(__float_as_uint(v) & 0xFFFFFF00u) | T8, T32};
      st8s((((w >> 2) * 2 + g) * 32 + 16 * (w & 1) + col) * 8, d, p.ws + WS_SG + (size_t)(hl * 4 + rg) * 1024);
    }
  }
  TS(3);
#pragma unroll
  for (int i = 4; i < 8; ++i) dma16kv(vok ^ ((i & 3) << 6), kbase + i * 1024, L_K + w * 8192 + i * 1024);
  u32x4 qp;
  const char* qsrc = p.ws + WS_SQ + (size_t)(h * 8 + w) * 2048 + b * 1024;
  ld16s(qp, lane * 16, qsrc);
  const u32 vov = g * 256 + ((((lane & 15) >> 1) ^ (g | (((w >> 1) & 1) << 2))) << 5) + ((lane & 1) << 4);
  const char* vbase = p.vc + kvrow + (size_t)w * 1024;
#pragma unroll
  for (int i = 0; i < 4; ++i) dma16kv(vov, vbase + i * 8192, L_V + w * 1024 + i * 8192);
  asm volatile("s_waitcnt vmcnt(4)" : "+v"(qp) :: "memory");
  TS(4);
  for (u32 spin = 0;; ++spin) {
    const u32 bad = (((qp[0] ^ T8) | (qp[2] ^ T8)) & 0xFFu) | (qp[1] ^ T32) | (qp[3] ^ T32);
    if (__builtin_amdgcn_ballot_w64(bad != 0) == 0 || spin > SPIN_CAP) break;
    ld16s(qp, lane * 16, qsrc);
    asm volatile("s_waitcnt vmcnt(0)" : "+v"(qp) :: "memory");
  }
  TS(5);
  lds_st<u32x2>(sm, L_ST + w * 512 + lane * 8, u32x2{qp[0] & 0xFFFFFF00u, qp[2] & 0xFFFFFF00u});
  BARRIER();
#pragma unroll
  for (int i = 4; i < 8; ++i) dma16kv(vov, vbase + i * 8192, L_V + w * 1024 + i * 8192);
  {
    f32x2 q2 = lds_ld<f32x2>(sm, L_ST + lane * 8);
#pragma unroll
    for (int j = 1; j < 8; ++j) q2 += lds_ld<f32x2>(sm, L_ST + j * 512 + lane * 8);
    lds_st<u32>(sm, L_QW + w * 256 + lane * 4, pkbf(q2[0], q2[1]));
    asm volatile("s_waitcnt lgkmcnt(0)" ::: "memory");
    const u32 kb = L_K + (32 * w + col) * 256 + ((g ^ col) << 4);
    f32x4 a0 = {0.f, 0.f, 0.f, 0.f}, a1 = a0;
#pragma unroll
    for (int t = 0; t < 4; ++t) {
      const bf16x8 aq = lds_ld<bf16x8>(sm, L_QW + w * 256 + t * 64 + g * 16);
      a0 = mfma(aq, lds_ld<bf16x8>(sm, kb ^ (t << 6)), a0);
      a1 = mfma(aq, lds_ld<bf16x8>(sm, (kb ^ (t << 6)) + 4096), a1);
    }
    const float s0 = (g & 2) ? a1[0] : a0[0];
    const float s2 = rbf(rbf(s0) * 0.08838834764831845f);
    float m = s2;
    m = fmaxf(m, dpp<ROR8>(m)); m = fmaxf(m, dpp<ROR4>(m)); m = fmaxf(m, dpp<ROR2>(m)); m = fmaxf(m, dpp<ROR1>(m));
    m = max32(m);
    const float e = ex2((s2 - m) * LOG2E);
    float l = e;
    l += dpp<ROR8>(l); l += dpp<ROR4>(l); l += dpp<ROR2>(l); l += dpp<ROR1>(l);
    l = sum32(l);
    if ((g & 1) == 0) lds_st<unsigned short>(sm, L_PM + w * 528 + (32 * w + 16 * (g >> 1) + col) * 2, (unsigned short)pkbf(e, e));
    if (lane < 2) lds_st<float>(sm, L_SM + lane * 32 + w * 4, lane ? l : m);
  }
  BARRIER();
  TS(6);
  {
    const u32 vo = (lane >> 3) * 1024 + vo128;
    const char* base = p.wo + (size_t)(h * 128 + 16 * w) * 1024 + sub * 128;
    dma16(vo, base, L_WO + (16 * w) * 128);
    dma16(vo ^ 64, base + 8 * 1024, L_WO + (16 * w + 8) * 128);
  }
  u32x4 gp = {0u, 0u, 0u, 0u};
  const u32 rd = (bid - 170) >> 1, cd = (bid - 170) & 1;
  const char* gsrc = p.ws + WS_SG + (size_t)(((w >> 2) * 43 + rd) * 4 + (w & 3)) * 1024;
  if (roleD) {
    ld16s(gp, lane * 16, gsrc);
    const u32 f0 = (lane >> 5) | (((w >> 1) & 1) << 2);
    const u32 vo = (lane >> 5) * 1024 + ((((lane & 31) >> 1) ^ f0) << 5) + ((lane & 1) << 4);
    const char* base = p.wd + (size_t)(32 * rd + 4 * w) * 1024 + cd * 512;
    dma16(vo, base, L_WD + (4 * w) * 512);
    dma16(vo ^ 64, base + 2 * 1024, L_WD + (4 * w + 2) * 512);
  }
  f32x4 cw; float Lw, Mw;
  {
    const f32x4 mA = lds_ld<f32x4>(sm, L_SM), mB = lds_ld<f32x4>(sm, L_SM + 16);
    const f32x4 ls = lds_ld<f32x4>(sm, L_SM + 32 + (g & 1) * 16);
    Mw = fmaxf(fmaxf(fmaxf(mA[0], mA[1]), fmaxf(mA[2], mA[3])), fmaxf(fmaxf(mB[0], mB[1]), fmaxf(mB[2], mB[3])));
    const f32x4 ms = (g & 1) ? mB : mA;
    float lp = 0.f;
#pragma unroll
    for (int k = 0; k < 4; ++k) { cw[k] = ex2((ms[k] - Mw) * LOG2E); lp = fmaf(cw[k], ls[k], lp); }
    Lw = sum16(lp);
  }
  bf16x8 ap[8];
#pragma unroll
  for (int t = 0; t < 8; ++t) ap[t] = lds_ld<bf16x8>(sm, L_PM + (col & 7) * 528 + t * 64 + g * 16);
  float gate = 0.f;
  const bool fin = bid < 128 && w == 4;
  if (fin) {
    const u32x4 xv = lds_ld<u32x4>(sm, L_XS + (bid >> 6) * 1024 + lane * 16), gv = lds_ld<u32x4>(sm, L_GW + lane * 16);
    float a = 0.f;
#pragma unroll
    for (int k = 0; k < 4; ++k) {
      a += rbf(__uint_as_float(xv[k] << 16) * __uint_as_float(gv[k] << 16));
      a += rbf(__uint_as_float(xv[k] & 0xFFFF0000u) * __uint_as_float(gv[k] & 0xFFFF0000u));
    }
    a += dpp<ROR8>(a); a += dpp<ROR4>(a); a += dpp<ROR2>(a); a += dpp<ROR1>(a);
    a = sum32(sum16(a));
    a = rbf(a);
    gate = rbf(1.f / (1.f + __expf(-a)));
  }
  const u32 vb = L_V + (8 * g + r4) * 256 + ((w ^ (r4 | ((g & 1) << 2))) << 5) + c4 * 8;
  f32x4 oa = {0.f, 0.f, 0.f, 0.f};
  if (roleD) WAITVM(7); else WAITVM(4);
  BARRIER();
#pragma unroll
  for (int t = 0; t < 6; ++t) oa = mfma(ap[t], cat8(trd(sm, vb + t * 8192), trd(sm, vb + t * 8192 + 1024)), oa);
  PIN(oa);
  if (roleD) WAITVM(5); else WAITVM(2);
  BARRIER();
  TS(7);
#pragma unroll
  for (int t = 6; t < 8; ++t) oa = mfma(ap[t], cat8(trd(sm, vb + t * 8192), trd(sm, vb + t * 8192 + 1024)), oa);
  {
    float v = oa[0] * cw[0];
    v = fmaf(oa[1], cw[1], v); v = fmaf(oa[2], cw[2], v); v = fmaf(oa[3], cw[3], v);
    v = sum16(v);
    const u32 pr = pkbf(v, dpp<QX1>(v));
    u32x2 d; u32 off;
    if (lane < 16) { d[0] = pr; off = (8 * w + (col >> 1)) * 8; }
    else { d[0] = (__float_as_uint(lane == 16 ? Mw : Lw) & 0xFFFFFF00u) | T8; off = 512 + (lane - 16) * 8; }
    d[1] = T32;
    if ((lane < 16 && (lane & 1) == 0) || (w == 0 && (lane == 16 || lane == 17)))
      st8s(off, d, p.ws + WS_SO + (size_t)(h * 8 + sub) * 640);
  }
  TS(8);
  WAITVM(1);
  if (roleD) {
    PIN(gp);
    for (u32 spin = 0;; ++spin) {
      const u32 bad = (((gp[0] ^ T8) | (gp[2] ^ T8)) & 0xFFu) | (gp[1] ^ T32) | (gp[3] ^ T32);
      if (__builtin_amdgcn_ballot_w64(bad != 0) == 0 || spin > SPIN_CAP) break;
      ld16s(gp, lane * 16, gsrc);
      asm volatile("s_waitcnt vmcnt(0)" : "+v"(gp) :: "memory");
    }
    lds_st<u32x2>(sm, L_ST + w * 512 + lane * 8, u32x2{gp[0] & 0xFFFFFF00u, gp[2] & 0xFFFFFF00u});
  }
  BARRIER();
  TS(9);
  u32x2 op; u32x4 sp;
  const char* osrc = p.ws + WS_SO + (size_t)(h * 8) * 640;
  const u32 soff = (((g & 1) << 2) | c4) * 640 + 512;
  bf16x8 wb[4];
  ld8s(op, w * 640 + lane * 8, osrc);
  ld16s(sp, soff, osrc);
  if (w < 4) {
    const u32 tb = L_WO + (8 * g + r4) * 128 + ((w ^ ((r4 >> 1) | ((g & 1) << 1))) << 5) + c4 * 8;
#pragma unroll
    for (int t = 0; t < 4; ++t) wb[t] = cat8(trd(sm, tb + t * 4096), trd(sm, tb + t * 4096 + 512));
  }
  asm volatile("s_waitcnt vmcnt(0)" : "+v"(op), "+v"(sp) :: "memory");
  for (u32 spin = 0;; ++spin) {
    const u32 bad = (op[1] ^ T32) | (((sp[0] ^ T8) | (sp[2] ^ T8)) & 0xFFu) | (sp[1] ^ T32) | (sp[3] ^ T32);
    if (__builtin_amdgcn_ballot_w64(bad != 0) == 0 || spin > SPIN_CAP) break;
    ld8s(op, w * 640 + lane * 8, osrc);
    ld16s(sp, soff, osrc);
    asm volatile("s_waitcnt vmcnt(0)" : "+v"(op), "+v"(sp) :: "memory");
  }
  TS(10);
  lds_st<u32>(sm, L_OS + w * 272 + lane * 4, op[0]);
  BARRIER();
  if (w < 4) {
    const float Mj = __uint_as_float(sp[0] & 0xFFFFFF00u), Lj = __uint_as_float(sp[2] & 0xFFFFFF00u);
    float Mb = fmaxf(Mj, dpp<QX1>(Mj)); Mb = fmaxf(Mb, dpp<QX2>(Mb));
    const float e = ex2((Mj - Mb) * LOG2E);
    float Lb = e * Lj; Lb += dpp<QX1>(Lb); Lb += dpp<QX2>(Lb);
    const float c = e / Lb;
    f32x4 acc = {0.f, 0.f, 0.f, 0.f};
#pragma unroll
    for (int t = 0; t < 4; ++t) acc = mfma(lds_ld<bf16x8>(sm, L_OS + (col & 7) * 272 + t * 64 + g * 16), wb[t], acc);
    float v = acc[0] * dpp<0x00>(c);
    v = fmaf(acc[1], dpp<0x55>(c), v); v = fmaf(acc[2], dpp<0xAA>(c), v); v = fmaf(acc[3], dpp<0xFF>(c), v);
    if (g < 2) {
      u32x2 d = {(__float_as_uint(v) & 0xFFFFFF00u) | T8, T32};
      st8s((g * 512 + 64 * sub + 16 * w + col) * 8, d, p.ws + WS_SF + (size_t)h * 8192);
    }
  } else {
    if (roleD) {
      const u32 w4 = w - 4;
      f32x2 v2 = lds_ld<f32x2>(sm, L_ST + lane * 8);
#pragma unroll
      for (int j = 1; j < 8; ++j) v2 += lds_ld<f32x2>(sm, L_ST + j * 512 + lane * 8);
      const u32 pr = pkbf(v2[0], v2[1]);
      auto sw = __builtin_amdgcn_permlane32_swap(pr, pr, false, false);
      const u32 gq = sw[0], uq = sw[1];
      const u32 hp = pkbf(__uint_as_float(gq << 16) * __uint_as_float(uq << 16), __uint_as_float(gq & 0xFFFF0000u) * __uint_as_float(uq & 0xFFFF0000u));
      lds_st<u32>(sm, L_QW + w * 256 + lane * 4, hp);
      asm volatile("s_waitcnt lgkmcnt(0)" ::: "memory");
      const bf16x8 ah = lds_ld<bf16x8>(sm, L_QW + w * 256 + (col & 1) * 64 + g * 16);
      const u32 tb = L_WD + (8 * g + r4) * 512 + (((4 * w4) ^ (r4 | ((g & 1) << 2))) << 5) + c4 * 8;
      f32x4 z = {0.f, 0.f, 0.f, 0.f}, acc[4];
#pragma unroll
      for (int q = 0; q < 4; ++q) acc[q] = mfma(ah, cat8(trd(sm, tb ^ (q << 5)), trd(sm, (tb ^ (q << 5)) + 2048)), z);
#pragma unroll
      for (int qq = 0; qq < 2; ++qq) {
        const f32x4 x0 = acc[2 * qq], x1 = acc[2 * qq + 1];
        const float lo = (g & 1) ? x0[1] : x0[0], hi = (g & 1) ? x1[1] : x1[0];
        const float v = (g & 2) ? hi : lo;
        u32x2 d = {(__float_as_uint(v) & 0xFFFFFF00u) | T8, T32};
        st8s(((g & 1) * 512 + 256 * cd + 16 * (4 * w4 + 2 * qq + (g >> 1)) + col) * 8, d, p.ws + WS_SB + (size_t)rd * 8192);
      }
    }
    if (fin) {
      const u32 fb = bid >> 6, n0 = (bid & 63) * 8;
      const u32 hh = lane >> 2;
      const u32 o0 = (fb * 512 + n0 + 2 * c4) * 8 + hh * 8192;
      const u32 hh2 = hh < 11 ? hh : 0u;
      const u32 o2 = (fb * 512 + n0 + 2 * c4) * 8 + (32 + hh2) * 8192;
      const char* fa = p.ws + WS_SF; const char* fbp = p.ws + WS_SB;
      u32x4 A0, A1, B0, B1, B2;
      for (u32 spin = 0;; ++spin) {
        ld16s(A0, o0, fa); ld16s(A1, o0, fa + 16 * 8192);
        ld16s(B0, o0, fbp); ld16s(B1, o0, fbp + 16 * 8192); ld16s(B2, o2, fbp);
        asm volatile("s_waitcnt vmcnt(0)" : "+v"(A0), "+v"(A1), "+v"(B0), "+v"(B1), "+v"(B2) :: "memory");
        const u32 lo = (A0[0] ^ T8) | (A0[2] ^ T8) | (A1[0] ^ T8) | (A1[2] ^ T8) | (B0[0] ^ T8) | (B0[2] ^ T8) | (B1[0] ^ T8) | (B1[2] ^ T8) | (B2[0] ^ T8) | (B2[2] ^ T8);
        const u32 hi = (A0[1] ^ T32) | (A0[3] ^ T32) | (A1[1] ^ T32) | (A1[3] ^ T32) | (B0[1] ^ T32) | (B0[3] ^ T32) | (B1[1] ^ T32) | (B1[3] ^ T32) | (B2[1] ^ T32) | (B2[3] ^ T32);
        if (__builtin_amdgcn_ballot_w64(((lo & 0xFFu) | hi) != 0) == 0 || spin > SPIN_CAP) break;
      }
      TS(11);
      auto val = [](u32 v) { return __uint_as_float(v & 0xFFFFFF00u); };
      float a0 = val(A0[0]) + val(A1[0]), a1 = val(A0[2]) + val(A1[2]);
      float b0 = val(B0[0]) + val(B1[0]), b1 = val(B0[2]) + val(B1[2]);
      if (hh < 11) { b0 += val(B2[0]); b1 += val(B2[2]); }
      a0 += dpp<ROR8>(a0); a1 += dpp<ROR8>(a1); b0 += dpp<ROR8>(b0); b1 += dpp<ROR8>(b1);
      a0 += dpp<ROR4>(a0); a1 += dpp<ROR4>(a1); b0 += dpp<ROR4>(b0); b1 += dpp<ROR4>(b1);
      a0 = sum32(sum16(a0)); a1 = sum32(sum16(a1)); b0 = sum32(sum16(b0)); b1 = sum32(sum16(b1));
      const float gm = rbf(1.f - gate);
      const float r0 = rbf(gate * rbf(a0)) + rbf(gm * rbf(b0)), r1 = rbf(gate * rbf(a1)) + rbf(gm * rbf(b1));
      if (lane < 4) *(u32*)(p.out + (fb * 512 + n0 + 2 * lane) * 2) = pkbf(r0, r1);
      if (bid == 0 && lane == 0) *(u32*)(p.ws + WS_EPOCH) = T32;
    }
  }
  TS(12);
}

extern "C" void kernel_launch(void* const* d_in, const int* in_sizes, int n_in, void* d_out, int out_size, void* d_ws, size_t ws_size, hipStream_t stream) {
  Params p;
  p.x = (const char*)d_in[0]; p.kc = (const char*)d_in[1]; p.vc = (const char*)d_in[2]; p.wq = (const char*)d_in[3]; p.wo = (const char*)d_in[4];
  p.wg = (const char*)d_in[6]; p.wu = (const char*)d_in[7]; p.wd = (const char*)d_in[8]; p.gw = (const char*)d_in[9];
  p.out = (char*)d_out; p.ws = (char*)d_ws;
  fused_decode<<<256, 512, 0, stream>>>(p);
}
```

```cpp
#include <hip/hip_runtime.h>
#include <hip/hip_bf16.h>
#include <stdint.h>

#define LAS __attribute__((address_space(3)))
typedef unsigned u32;
typedef unsigned long long u64;
typedef unsigned u32x4 __attribute__((ext_vector_type(4)));
typedef unsigned u32x2 __attribute__((ext_vector_type(2)));
typedef float f32x4 __attribute__((ext_vector_type(4)));
typedef float f32x2 __attribute__((ext_vector_type(2)));
typedef short s16x4 __attribute__((ext_vector_type(4)));
typedef short s16x8 __attribute__((ext_vector_type(8)));
typedef __bf16 bf16x8 __attribute__((ext_vector_type(8)));
typedef __bf16 bf16x2 __attribute__((ext_vector_type(2)));

#define KV_NT " nt"
#define W_NT " nt"

constexpr u32 WS_EPOCH = 0;
constexpr u32 WS_SQ = 4096;
constexpr u32 WS_SG = WS_SQ + 32 * 8 * 256 * 8;
constexpr u32 WS_SO = WS_SG + 86 * 4 * 128 * 8;
constexpr u32 WS_SF = WS_SO + 32 * 8 * 640;
constexpr u32 WS_SB = WS_SF + 32 * 1024 * 8;
constexpr u32 WS_END = WS_SB + 43 * 1024 * 8;

constexpr u32 L_K = 0, L_WO = 0, L_WD = 16384;
constexpr u32 L_V = 65536, L_WQ = L_V + 32768, L_GU = L_V + 49152;
constexpr u32 L_XS = 131072;
constexpr u32 L_GW = L_XS + 2048;
constexpr u32 L_EP = L_GW + 1024;
constexpr u32 L_ST = L_EP + 1024;
constexpr u32 L_QW = L_ST + 4096;
constexpr u32 L_PM = L_QW + 2048;
constexpr u32 L_SM = L_PM + 8 * 528;
constexpr u32 L_OS = L_SM + 64;
constexpr u32 L_END = L_OS + 8 * 272;
typedef char lds_map_fits[L_END <= 163840 ? 1 : -1];

struct Params {
  const char* x; const char* kc; const char* vc; const char* wq; const char* wo;
  const char* wg; const char* wu; const char* wd; const char* gw; char* out; char* ws; u64 unused;
};
typedef u32 u32x16 __attribute__((ext_vector_type(16)));
typedef u32 u32x8 __attribute__((ext_vector_type(8)));
template <typename T> __device__ __forceinline__ T mkptr(u32 lo, u32 hi) { return (T)(((u64)hi << 32) | lo); }

__device__ __forceinline__ void dma16(u32 voff, const char* sbase, u32 lds) {
  asm volatile("s_mov_b32 m0, %2\n\ts_nop 0\n\tglobal_load_lds_dwordx4 %0, %1" W_NT :: "v"(voff), "s"(sbase), "s"(lds) : "memory");
}
__device__ __forceinline__ void dma16kv(u32 voff, const char* sbase, u32 lds) {
  asm volatile("s_mov_b32 m0, %2\n\ts_nop 0\n\tglobal_load_lds_dwordx4 %0, %1" KV_NT :: "v"(voff), "s"(sbase), "s"(lds) : "memory");
}
__device__ __forceinline__ void dma4kv(u32 v0, u32 v1, u32 v2, u32 v3, const char* sbase, u32 lds) {
  asm volatile("s_mov_b32 m0, %5\n\ts_nop 0\n\tglobal_load_lds_dwordx4 %0, %4" KV_NT "\n\tglobal_load_lds_dwordx4 %1, %4 offset:1024" KV_NT
               "\n\tglobal_load_lds_dwordx4 %2, %4 offset:2048" KV_NT "\n\tglobal_load_lds_dwordx4 %3, %4 offset:3072" KV_NT
               :: "v"(v0), "v"(v1), "v"(v2), "v"(v3), "s"(sbase), "s"(lds) : "memory");
}
__device__ __forceinline__ void dma16p(u32 voff, const char* sbase, u32 lds) {
  asm volatile("s_mov_b32 m0, %2\n\ts_nop 0\n\tglobal_load_lds_dwordx4 %0, %1 sc1" :: "v"(voff), "s"(sbase), "s"(lds) : "memory");
}
__device__ __forceinline__ void ld16s(u32x4& d, u32 voff, const char* sbase) {
  asm volatile("global_load_dwordx4 %0, %1, %2 sc1" : "=v"(d) : "v"(voff), "s"(sbase) : "memory");
}
__device__ __forceinline__ void st8s(u32 voff, u32x2 d, char* sbase) {
  asm volatile("global_store_dwordx2 %0, %1, %2 sc1" :: "v"(voff), "v"(d), "s"(sbase) : "memory");
}
#define WAITVM(n) asm volatile("s_waitcnt vmcnt(" #n ")" ::: "memory")
#define BARRIER() asm volatile("s_waitcnt lgkmcnt(0)\n\ts_barrier" ::: "memory")
#define PIN(x) asm volatile("" : "+v"(x))

template <typename T> __device__ __forceinline__ T lds_ld(const LAS char* base, u32 off) { return *(const LAS T*)(base + off); }
template <typename T> __device__ __forceinline__ void lds_st(LAS char* base, u32 off, T v) { *(LAS T*)(base + off) = v; }
__device__ __forceinline__ s16x4 trd(const LAS char* base, u32 off) {
  return __builtin_bit_cast(s16x4, __builtin_amdgcn_ds_read_tr16_b64_v4i16((LAS s16x4*)(base + off)));
}
__device__ __forceinline__ bf16x8 cat8(s16x4 a, s16x4 b) {
  s16x8 r = __builtin_shufflevector(a, b, 0, 1, 2, 3, 4, 5, 6, 7); return __builtin_bit_cast(bf16x8, r);
}
__device__ __forceinline__ f32x4 mfma(bf16x8 a, bf16x8 b, f32x4 c) { return __builtin_amdgcn_mfma_f32_16x16x32_bf16(a, b, c, 0, 0, 0); }
__device__ __forceinline__ u32 pkbf(float lo, float hi) {
  f32x2 v = {lo, hi}; return __builtin_bit_cast(u32, __builtin_convertvector(v, bf16x2));
}
__device__ __forceinline__ float rbf(float v) { return __uint_as_float(pkbf(v, v) << 16); }
template <int C> __device__ __forceinline__ float dpp(float v) {
  return __builtin_bit_cast(float, __builtin_amdgcn_mov_dpp(__builtin_bit_cast(int, v), C, 0xf, 0xf, true));
}
constexpr int ROR1 = 0x121, ROR2 = 0x122, ROR4 = 0x124, ROR8 = 0x128, QX1 = 0xB1, QX2 = 0x4E;
__device__ __forceinline__ float sum16(float v) { auto s = __builtin_amdgcn_permlane16_swap(__float_as_uint(v), __float_as_uint(v), false, false); return __uint_as_float(s[0]) + __uint_as_float(s[1]); }
__device__ __forceinline__ float sum32(float v) { auto s = __builtin_amdgcn_permlane32_swap(__float_as_uint(v), __float_as_uint(v), false, false); return __uint_as_float(s[0]) + __uint_as_float(s[1]); }
__device__ __forceinline__ float max16(float v) { auto s = __builtin_amdgcn_permlane16_swap(__float_as_uint(v), __float_as_uint(v), false, false); return fmaxf(__uint_as_float(s[0]), __uint_as_float(s[1])); }
__device__ __forceinline__ float max32(float v) { auto s = __builtin_amdgcn_permlane32_swap(__float_as_uint(v), __float_as_uint(v), false, false); return fmaxf(__uint_as_float(s[0]), __uint_as_float(s[1])); }
__device__ __forceinline__ float ex2(float v) { return __builtin_amdgcn_exp2f(v); }
constexpr float LOG2E = 1.4426950408889634f;
constexpr u32 SPIN_CAP = 1u << 16;

#define GCHECK "v_xor_b32 %[t0], %[T8], %[q0]\n\tv_xor_b32 %[t1], %[T8], %[q2]\n\tv_or_b32 %[t0], %[t0], %[t1]\n\tv_and_b32 %[t0], 0xff, %[t0]\n\t" \
               "v_xor_b32 %[t1], %[T32], %[q1]\n\tv_or_b32 %[t0], %[t0], %[t1]\n\tv_xor_b32 %[t1], %[T32], %[q3]\n\tv_or_b32 %[t0], %[t0], %[t1]\n\t" \
               "v_cmp_ne_u32 vcc, 0, %[t0]\n\t"

__global__ void __launch_bounds__(512, 2) fused_decode(Params unused_) {
  Params p;
  {
    u32x16 A; u32x8 B;
    asm volatile("s_load_dwordx16 %0, %2, 0x0\n\ts_load_dwordx8 %1, %2, 0x40\n\ts_waitcnt lgkmcnt(0)" : "=&s"(A), "=&s"(B) : "s"(__builtin_amdgcn_kernarg_segment_ptr()));
    p.x = mkptr<const char*>(A[0], A[1]); p.kc = mkptr<const char*>(A[2], A[3]); p.vc = mkptr<const char*>(A[4], A[5]); p.wq = mkptr<const char*>(A[6], A[7]);
    p.wo = mkptr<const char*>(A[8], A[9]); p.wg = mkptr<const char*>(A[10], A[11]); p.wu = mkptr<const char*>(A[12], A[13]); p.wd = mkptr<const char*>(A[14], A[15]);
    p.gw = mkptr<const char*>(B[0], B[1]); p.out = mkptr<char*>(B[2], B[3]); p.ws = mkptr<char*>(B[4], B[5]);
  }
  __shared__ __attribute__((aligned(1024))) char smem_[L_END];
  LAS char* sm = (LAS char*)smem_;
  const u32 tid = threadIdx.x, lane = tid & 63;
  const u32 w = __builtin_amdgcn_readfirstlane(tid >> 6);
  const u32 bid = blockIdx.x;
  const u32 h = bid >> 3, sub = (bid + h) & 7, b = sub >> 2, s = sub & 3;
  const bool roleG = bid < 172, roleD = bid >= 170;
  const u32 g = lane >> 4, col = lane & 15, r4 = (lane >> 2) & 3, c4 = lane & 3;
  const u32 hf = (lane & 1) << 4, un = (lane & 15) >> 1;
  {
    const u32 vo = g * 8192 + ((un ^ (g | ((w & 1) << 2))) << 5) + hf;
    const char* base = p.wq + (size_t)(64 * sub + 8 * w) * 8192 + h * 256;
    dma16(vo, base, L_WQ + w * 2048);
    dma16(vo + 4 * 8192, base, L_WQ + w * 2048 + 1024);
  }
  if (w < 4) dma16p(lane * 16, w == 0 ? p.x : w == 1 ? p.x + 1024 : w == 2 ? p.gw : p.ws + WS_EPOCH, L_XS + w * 1024);
  const u32 vo128 = ((((lane & 7) >> 1) ^ ((lane >> 4) & 1)) << 5) + hf;
  if (roleG) {
    const u32 ell = bid >> 2, rg = bid & 3;
    const u32 vo = (lane >> 3) * 5504 + (vo128 ^ ((w & 1) << 6));
    const u32 o = (64 * rg + 8 * w) * 5504 + ell * 128;
    dma16(vo, p.wg + o, L_GU + w * 1024);
    dma16(vo, p.wu + o, L_GU + 8192 + w * 1024);
  }
  const size_t kvrow = ((size_t)(b * 32 + h) * 4096 + 3072 + 256 * s) * 256;
  const u32 vok = g * 256 + ((col ^ g) << 4);
  const char* kbase = p.kc + kvrow + (size_t)w * 8192;
  dma4kv(vok, vok ^ 64, vok ^ 128, vok ^ 192, kbase, L_K + w * 8192);
  lds_st<u32x2>(sm, L_PM + w * 528 + lane * 8, u32x2{0u, 0u});
  if (roleG) WAITVM(6); else WAITVM(4);
  BARRIER();
  const u32 T32 = __builtin_amdgcn_readfirstlane(lds_ld<u32>(sm, L_EP)) + 1u;
  const u32 T8 = (T32 ^ 0x5Au) & 0xFFu;
  {
    const u32 xa = L_XS + (col & 1) * 1024 + (64 * sub + 8 * g) * 2;
    const u32 tb = L_WQ + (8 * g + r4) * 256 + ((w ^ (r4 | ((g & 1) << 2))) << 5) + c4 * 8;
    f32x4 acc = {0.f, 0.f, 0.f, 0.f};
#pragma unroll
    for (int t = 0; t < 2; ++t) {
      const bf16x8 a = lds_ld<bf16x8>(sm, xa + t * 64);
      const bf16x8 bb = cat8(trd(sm, tb + t * 8192), trd(sm, tb + t * 8192 + 1024));
      acc = mfma(a, bb, acc);
    }
    const float v = (g & 1) ? acc[1] : acc[0];
    if (g < 2) {
      u32x2 d = {(__float_as_uint(v) & 0xFFFFFF00u) | T8, T32};
      st8s((g * 128 + 16 * w + col) * 8, d, p.ws + WS_SQ + (size_t)(h * 8 + sub) * 2048);
    }
  }
  if (roleG) {
    WAITVM(5);
    BARRIER();
    const u32 ell = bid >> 2, rg = bid & 3;
    const u32 hl = 2 * ell + ((w & 3) >> 1);
    const u32 sel = hl >= 43 ? 0x07060302u : 0x05040100u;
    const u32 xa = L_XS + (col & 1) * 1024 + (128 * rg + 16 * g) * 2;
    const u32 tb = L_GU + (w >> 2) * 8192 + (8 * g + r4) * 128 + (((w & 3) ^ ((r4 >> 1) | ((g & 1) << 1))) << 5) + c4 * 8;
    f32x4 acc = {0.f, 0.f, 0.f, 0.f};
#pragma unroll
    for (int t = 0; t < 2; ++t) {
      const u32x4 x0 = lds_ld<u32x4>(sm, xa + t * 128), x1 = lds_ld<u32x4>(sm, xa + t * 128 + 16);
      const u32x4 a = {__builtin_amdgcn_perm(x0[1], x0[0], sel), __builtin_amdgcn_perm(x0[3], x0[2], sel),
                       __builtin_amdgcn_perm(x1[1], x1[0], sel), __builtin_amdgcn_perm(x1[3], x1[2], sel)};
      const bf16x8 bb = cat8(trd(sm, tb + t * 4096), trd(sm, tb + t * 4096 + 512));
      acc = mfma(__builtin_bit_cast(bf16x8, a), bb, acc);
    }
    const float v = (g & 1) ? acc[1] : acc[0];
    if (g < 2) {
      u32x2 d = {(__float_as_uint(v) & 0xFFFFFF00u) | T8, T32};
      st8s((((w >> 2) * 2 + g) * 32 + 16 * (w & 1) + col) * 8, d, p.ws + WS_SG + (size_t)(hl * 4 + rg) * 1024);
    }
  }
  dma4kv(vok, vok ^ 64, vok ^ 128, vok ^ 192, kbase + 4096, L_K + w * 8192 + 4096);
  const char* qsrc = p.ws + WS_SQ + (size_t)(h * 8 + w) * 2048 + b * 1024;
  asm volatile("global_load_dwordx4 v[104:107], %0, %1 sc1" :: "v"(lane * 16), "s"(qsrc) : "memory", "v104", "v105", "v106", "v107");
  const u32 vov = g * 256 + ((((lane & 15) >> 1) ^ (g | (((w >> 1) & 1) << 2))) << 5) + ((lane & 1) << 4);
  const char* vbase = p.vc + kvrow + (size_t)w * 1024;
#pragma unroll
  for (int i = 0; i < 4; ++i) dma16kv(vov, vbase + i * 8192, L_V + w * 1024 + i * 8192);
  float gate = 0.f;
  const bool fin = bid < 128 && w == 4;
  if (fin) {
    const u32x4 xv = lds_ld<u32x4>(sm, L_XS + (bid >> 6) * 1024 + lane * 16), gv = lds_ld<u32x4>(sm, L_GW + lane * 16);
    float a = 0.f;
#pragma unroll
    for (int k = 0; k < 4; ++k) {
      a += rbf(__uint_as_float(xv[k] << 16) * __uint_as_float(gv[k] << 16));
      a += rbf(__uint_as_float(xv[k] & 0xFFFF0000u) * __uint_as_float(gv[k] & 0xFFFF0000u));
    }
    a += dpp<ROR8>(a); a += dpp<ROR4>(a); a += dpp<ROR2>(a); a += dpp<ROR1>(a);
    a = sum32(sum16(a));
    a = rbf(a);
    gate = rbf(1.f / (1.f + __expf(-a)));
  }
  PIN(gate);
  {
    u32 q0, q1, q2, q3, t0, t1, cnt;
    asm volatile(
        "s_waitcnt vmcnt(4)\n\t"
        "s_mov_b32 %[cnt], 0\n"
        "1:\n\t"
        "v_mov_b32 %[q0], v104\n\tv_mov_b32 %[q1], v105\n\tv_mov_b32 %[q2], v106\n\tv_mov_b32 %[q3], v107\n\t"
        GCHECK
        "s_cbranch_vccz 9f\n\t"
        "s_add_u32 %[cnt], %[cnt], 1\n\ts_cmp_gt_u32 %[cnt], 0x10000\n\ts_cbranch_scc1 9f\n\t"
        "global_load_dwordx4 v[104:107], %[off], %[src] sc1\n\t"
        "s_waitcnt vmcnt(0)\n\t"
        "s_branch 1b\n"
        "9:\n\t"
        : [q0] "=&v"(q0), [q1] "=&v"(q1), [q2] "=&v"(q2), [q3] "=&v"(q3), [t0] "=&v"(t0), [t1] "=&v"(t1), [cnt] "=&s"(cnt)
        : [off] "v"(lane * 16), [src] "s"(qsrc), [T8] "s"(T8), [T32] "s"(T32)
        : "memory", "scc", "vcc", "v104", "v105", "v106", "v107");
    lds_st<u32x2>(sm, L_ST + w * 512 + lane * 8, u32x2{q0 & 0xFFFFFF00u, q2 & 0xFFFFFF00u});
  }
  BARRIER();
#pragma unroll
  for (int i = 4; i < 8; ++i) dma16kv(vov, vbase + i * 8192, L_V + w * 1024 + i * 8192);
  {
    f32x2 q2 = lds_ld<f32x2>(sm, L_ST + lane * 8);
#pragma unroll
    for (int j = 1; j < 8; ++j) q2 += lds_ld<f32x2>(sm, L_ST + j * 512 + lane * 8);
    lds_st<u32>(sm, L_QW + w * 256 + lane * 4, pkbf(q2[0], q2[1]));
    asm volatile("s_waitcnt lgkmcnt(0)" ::: "memory");
    const u32 kb = L_K + (32 * w + col) * 256 + ((g ^ col) << 4);
    f32x4 a0 = {0.f, 0.f, 0.f, 0.f}, a1 = a0;
#pragma unroll
    for (int t = 0; t < 4; ++t) {
      const bf16x8 aq = lds_ld<bf16x8>(sm, L_QW + w * 256 + t * 64 + g * 16);
      a0 = mfma(aq, lds_ld<bf16x8>(sm, kb ^ (t << 6)), a0);
      a1 = mfma(aq, lds_ld<bf16x8>(sm, (kb ^ (t << 6)) + 4096), a1);
    }
    const float s0 = (g & 2) ? a1[0] : a0[0];
    const float s2 = rbf(rbf(s0) * 0.08838834764831845f);
    float m = s2;
    m = fmaxf(m, dpp<ROR8>(m)); m = fmaxf(m, dpp<ROR4>(m)); m = fmaxf(m, dpp<ROR2>(m)); m = fmaxf(m, dpp<ROR1>(m));
    m = max32(m);
    const float e = ex2((s2 - m) * LOG2E);
    float l = e;
    l += dpp<ROR8>(l); l += dpp<ROR4>(l); l += dpp<ROR2>(l); l += dpp<ROR1>(l);
    l = sum32(l);
    if ((g & 1) == 0) lds_st<unsigned short>(sm, L_PM + w * 528 + (32 * w + 16 * (g >> 1) + col) * 2, (unsigned short)pkbf(e, e));
    if (lane < 2) lds_st<float>(sm, L_SM + lane * 32 + w * 4, lane ? l : m);
  }
  BARRIER();
  {
    const u32 vo = (lane >> 3) * 1024 + vo128;
    const char* base = p.wo + (size_t)(h * 128 + 16 * w) * 1024 + sub * 128;
    dma16(vo, base, L_WO + (16 * w) * 128);
    dma16(vo ^ 64, base + 8 * 1024, L_WO + (16 * w + 8) * 128);
  }
  const u32 rd = (bid - 170) >> 1, cd = (bid - 170) & 1;
  const char* gsrc = p.ws + WS_SG + (size_t)(((w >> 2) * 43 + rd) * 4 + (w & 3)) * 1024;
  if (roleD) {
    asm volatile("global_load_dwordx4 v[108:111], %0, %1 sc1" :: "v"(lane * 16), "s"(gsrc) : "memory", "v108", "v109", "v110", "v111");
    const u32 f0 = (lane >> 5) | (((w >> 1) & 1) << 2);
    const u32 vo = (lane >> 5) * 1024 + ((((lane & 31) >> 1) ^ f0) << 5) + ((lane & 1) << 4);
    const char* base = p.wd + (size_t)(32 * rd + 4 * w) * 1024 + cd * 512;
    dma16(vo, base, L_WD + (4 * w) * 512);
    dma16(vo ^ 64, base + 2 * 1024, L_WD + (4 * w + 2) * 512);
  }
  float cw, Lw, Mw;
  {
    const u32 j4 = (((g & 1) << 2) | c4) * 4;
    const float mj = lds_ld<float>(sm, L_SM + j4), lj = lds_ld<float>(sm, L_SM + 32 + j4);
    float M = fmaxf(mj, dpp<QX1>(mj)); M = fmaxf(M, dpp<QX2>(M));
    Mw = max16(M);
    cw = ex2((mj - Mw) * LOG2E);
    float lp = cw * lj; lp += dpp<QX1>(lp); lp += dpp<QX2>(lp);
    Lw = sum16(lp);
  }
  bf16x8 ap[8];
#pragma unroll
  for (int t = 0; t < 8; ++t) ap[t] = lds_ld<bf16x8>(sm, L_PM + (col & 7) * 528 + t * 64 + g * 16);
  const u32 vb = L_V + (8 * g + r4) * 256 + ((w ^ (r4 | ((g & 1) << 2))) << 5) + c4 * 8;
  u32 ooff = lane < 16 ? (8 * w + (col >> 1)) * 8 : 512 + (lane - 16) * 8;
  u32 ostat = (__float_as_uint(lane == 16 ? Mw : Lw) & 0xFFFFFF00u) | T8;
  const u64 omask = 0x5555ull | (w == 0 ? 0x30000ull : 0ull);
  char* odst = p.ws + WS_SO + (size_t)(h * 8 + sub) * 640;
  float cw0 = dpp<0x00>(cw), cw1 = dpp<0x55>(cw), cw2 = dpp<0xAA>(cw), cw3 = dpp<0xFF>(cw);
  PIN(ooff); PIN(ostat); PIN(cw0); PIN(cw1); PIN(cw2); PIN(cw3);
  f32x4 oa = {0.f, 0.f, 0.f, 0.f};
  if (roleD) WAITVM(7); else WAITVM(4);
  BARRIER();
#pragma unroll
  for (int t = 0; t < 6; ++t) oa = mfma(ap[t], cat8(trd(sm, vb + t * 8192), trd(sm, vb + t * 8192 + 1024)), oa);
  PIN(oa);
  if (roleD) WAITVM(5); else WAITVM(2);
  BARRIER();
#pragma unroll
  for (int t = 6; t < 8; ++t) oa = mfma(ap[t], cat8(trd(sm, vb + t * 8192), trd(sm, vb + t * 8192 + 1024)), oa);
  {
    float v = oa[0] * cw0;
    v = fmaf(oa[1], cw1, v); v = fmaf(oa[2], cw2, v); v = fmaf(oa[3], cw3, v);
    v = sum16(v);
    const u32 pr = pkbf(v, dpp<QX1>(v));
    u32x2 d = {lane < 16 ? pr : ostat, T32};
    asm volatile("s_mov_b64 exec, %3\n\tglobal_store_dwordx2 %0, %1, %2 sc1\n\ts_mov_b64 exec, -1" :: "v"(ooff), "v"(d), "s"(odst), "s"(omask) : "memory");
  }
  WAITVM(1);
  if (roleD) {
    u32 q0, q1, q2, q3, t0, t1, cnt;
    asm volatile(
        "s_mov_b32 %[cnt], 0\n"
        "1:\n\t"
        "v_mov_b32 %[q0], v108\n\tv_mov_b32 %[q1], v109\n\tv_mov_b32 %[q2], v110\n\tv_mov_b32 %[q3], v111\n\t"
        GCHECK
        "s_cbranch_vccz 9f\n\t"
        "s_add_u32 %[cnt], %[cnt], 1\n\ts_cmp_gt_u32 %[cnt], 0x10000\n\ts_cbranch_scc1 9f\n\t"
        "global_load_dwordx4 v[108:111], %[off], %[src] sc1\n\t"
        "s_waitcnt vmcnt(0)\n\t"
        "s_branch 1b\n"
        "9:\n\t"
        : [q0] "=&v"(q0), [q1] "=&v"(q1), [q2] "=&v"(q2), [q3] "=&v"(q3), [t0] "=&v"(t0), [t1] "=&v"(t1), [cnt] "=&s"(cnt)
        : [off] "v"(lane * 16), [src] "s"(gsrc), [T8] "s"(T8), [T32] "s"(T32)
        : "memory", "scc", "vcc", "v108", "v109", "v110", "v111");
    lds_st<u32x2>(sm, L_ST + w * 512 + lane * 8, u32x2{q0 & 0xFFFFFF00u, q2 & 0xFFFFFF00u});
  }
  BARRIER();
  if (w < 4) __builtin_amdgcn_s_setprio(2);
  const char* osrc = p.ws + WS_SO + (size_t)(h * 8) * 640;
  const u32 soff = (((g & 1) << 2) | c4) * 640 + 512;
  bf16x8 wb[4];
  if (w < 4) {
    const u32 tb = L_WO + (8 * g + r4) * 128 + ((w ^ ((r4 >> 1) | ((g & 1) << 1))) << 5) + c4 * 8;
#pragma unroll
    for (int t = 0; t < 4; ++t) wb[t] = cat8(trd(sm, tb + t * 4096), trd(sm, tb + t * 4096 + 512));
  } else {
    if (roleD) {
      const u32 w4 = w - 4;
      f32x2 v2 = lds_ld<f32x2>(sm, L_ST + lane * 8);
#pragma unroll
      for (int j = 1; j < 8; ++j) v2 += lds_ld<f32x2>(sm, L_ST + j * 512 + lane * 8);
      const u32 pr = pkbf(v2[0], v2[1]);
      auto sw = __builtin_amdgcn_permlane32_swap(pr, pr, false, false);
      const u32 gq = sw[0], uq = sw[1];
      const u32 hp = pkbf(__uint_as_float(gq << 16) * __uint_as_float(uq << 16), __uint_as_float(gq & 0xFFFF0000u) * __uint_as_float(uq & 0xFFFF0000u));
      lds_st<u32>(sm, L_QW + w * 256 + lane * 4, hp);
      asm volatile("s_waitcnt lgkmcnt(0)" ::: "memory");
      const bf16x8 ah = lds_ld<bf16x8>(sm, L_QW + w * 256 + (col & 1) * 64 + g * 16);
      const u32 tb = L_WD + (8 * g + r4) * 512 + (((4 * w4) ^ (r4 | ((g & 1) << 2))) << 5) + c4 * 8;
      f32x4 z = {0.f, 0.f, 0.f, 0.f}, acc[4];
#pragma unroll
      for (int q = 0; q < 4; ++q) acc[q] = mfma(ah, cat8(trd(sm, tb ^ (q << 5)), trd(sm, (tb ^ (q << 5)) + 2048)), z);
#pragma unroll
      for (int qq = 0; qq < 2; ++qq) {
        const f32x4 x0 = acc[2 * qq], x1 = acc[2 * qq + 1];
        const float lo = (g & 1) ? x0[1] : x0[0], hi = (g & 1) ? x1[1] : x1[0];
        const float v = (g & 2) ? hi : lo;
        u32x2 d = {(__float_as_uint(v) & 0xFFFFFF00u) | T8, T32};
        st8s(((g & 1) * 512 + 256 * cd + 16 * (4 * w4 + 2 * qq + (g >> 1)) + col) * 8, d, p.ws + WS_SB + (size_t)rd * 8192);
      }
    }
  }
  u32 foff = ((g & 1) * 512 + 64 * sub + 16 * w + col) * 8, aoff = L_OS + (col & 7) * 272 + g * 16;
  char* fdst = p.ws + WS_SF + (size_t)h * 8192;
  PIN(foff); PIN(aoff);
  u32 sM, sL;
  {
    u32 t0, t1, cnt;
    u32 ooffr = w * 640 + lane * 8, osta = L_OS + w * 272 + lane * 4;
    asm volatile(
        "s_mov_b32 %[cnt], 0\n"
        "1:\n\t"
        "global_load_dwordx2 v[104:105], %[oo], %[src] sc1\n\t"
        "global_load_dwordx4 v[108:111], %[so], %[src] sc1\n\t"
        "s_waitcnt vmcnt(0)\n\t"
        "v_xor_b32 %[t0], %[T8], v108\n\tv_xor_b32 %[t1], %[T8], v110\n\tv_or_b32 %[t0], %[t0], %[t1]\n\tv_and_b32 %[t0], 0xff, %[t0]\n\t"
        "v_xor_b32 %[t1], %[T32], v105\n\tv_or_b32 %[t0], %[t0], %[t1]\n\tv_xor_b32 %[t1], %[T32], v109\n\tv_or_b32 %[t0], %[t0], %[t1]\n\t"
        "v_xor_b32 %[t1], %[T32], v111\n\tv_or_b32 %[t0], %[t0], %[t1]\n\t"
        "v_cmp_ne_u32 vcc, 0, %[t0]\n\ts_cbranch_vccz 9f\n\t"
        "s_add_u32 %[cnt], %[cnt], 1\n\ts_cmp_lt_u32 %[cnt], 0x10000\n\ts_cbranch_scc1 1b\n"
        "9:\n\t"
        "ds_write_b32 %[osta], v104\n\t"
        "v_and_b32 %[sM], 0xffffff00, v108\n\tv_and_b32 %[sL], 0xffffff00, v110\n\ts_nop 1\n\t"
        : [sM] "=&v"(sM), [sL] "=&v"(sL), [t0] "=&v"(t0), [t1] "=&v"(t1), [cnt] "=&s"(cnt)
        : [oo] "v"(ooffr), [so] "v"(soff), [src] "s"(osrc), [osta] "v"(osta), [T8] "s"(T8), [T32] "s"(T32)
        : "memory", "scc", "vcc", "v104", "v105", "v108", "v109", "v110", "v111");
  }
  BARRIER();
  if (w < 4) {
    bf16x8 a0 = lds_ld<bf16x8>(sm, aoff), a1 = lds_ld<bf16x8>(sm, aoff + 64), a2 = lds_ld<bf16x8>(sm, aoff + 128), a3 = lds_ld<bf16x8>(sm, aoff + 192);
    __builtin_amdgcn_sched_barrier(0);
    float c;
    {
      const float Mj = __uint_as_float(sM), Lj = __uint_as_float(sL);
      float Mb = fmaxf(Mj, dpp<QX1>(Mj)); Mb = fmaxf(Mb, dpp<QX2>(Mb));
      const float e = ex2((Mj - Mb) * LOG2E);
      float Lb = e * Lj; Lb += dpp<QX1>(Lb); Lb += dpp<QX2>(Lb);
      c = e * __builtin_amdgcn_rcpf(Lb);
    }
    float c0 = dpp<0x00>(c), c1 = dpp<0x55>(c), c2 = dpp<0xAA>(c), c3 = dpp<0xFF>(c);
    asm volatile("" : "+v"(a0), "+v"(a1), "+v"(a2), "+v"(a3), "+v"(c0), "+v"(c1), "+v"(c2), "+v"(c3));
    const f32x4 z = {0.f, 0.f, 0.f, 0.f};
    f32x4 acc = mfma(a0, wb[0], z), acc2 = mfma(a2, wb[2], z);
    acc = mfma(a1, wb[1], acc); acc2 = mfma(a3, wb[3], acc2);
    acc += acc2;
    float v = acc[0] * c0;
    v = fmaf(acc[1], c1, v); v = fmaf(acc[2], c2, v); v = fmaf(acc[3], c3, v);
    const u32x2 d = {(__float_as_uint(v) & 0xFFFFFF00u) | T8, T32};
    asm volatile("s_mov_b64 exec, %3\n\tglobal_store_dwordx2 %0, %1, %2 sc1\n\ts_mov_b64 exec, -1" :: "v"(foff), "v"(d), "s"(fdst), "s"(0xFFFFFFFFull) : "memory");
  } else {
    if (fin) {
      const u32 fb = bid >> 6, n0 = (bid & 63) * 8;
      const u32 hh = lane >> 2;
      const u32 o0 = (fb * 512 + n0 + 2 * c4) * 8 + hh * 8192;
      const u32 hh2 = hh < 11 ? hh : 0u;
      const u32 o2 = (fb * 512 + n0 + 2 * c4) * 8 + (32 + hh2) * 8192;
      const char* fa = p.ws + WS_SF; const char* fbp = p.ws + WS_SB;
      u32 outoff = (fb * 512 + n0 + 2 * lane) * 2;
      PIN(outoff);
      u32x4 A0, A1, B0, B1, B2;
      for (u32 spin = 0;; ++spin) {
        ld16s(B0, o0, fbp); ld16s(B1, o0, fbp + 16 * 8192); ld16s(B2, o2, fbp);
        asm volatile("s_waitcnt vmcnt(0)" : "+v"(B0), "+v"(B1), "+v"(B2) :: "memory");
        const u32 lo = (B0[0] ^ T8) | (B0[2] ^ T8) | (B1[0] ^ T8) | (B1[2] ^ T8) | (B2[0] ^ T8) | (B2[2] ^ T8);
        const u32 hi = (B0[1] ^ T32) | (B0[3] ^ T32) | (B1[1] ^ T32) | (B1[3] ^ T32) | (B2[1] ^ T32) | (B2[3] ^ T32);
        if (__builtin_amdgcn_ballot_w64(((lo & 0xFFu) | hi) != 0) == 0 || spin > SPIN_CAP) break;
      }
      auto val = [](u32 v) { return __uint_as_float(v & 0xFFFFFF00u); };
      float b0 = val(B0[0]) + val(B1[0]), b1 = val(B0[2]) + val(B1[2]);
      if (hh < 11) { b0 += val(B2[0]); b1 += val(B2[2]); }
      b0 += dpp<ROR8>(b0); b1 += dpp<ROR8>(b1);
      b0 += dpp<ROR4>(b0); b1 += dpp<ROR4>(b1);
      b0 = sum32(sum16(b0)); b1 = sum32(sum16(b1));
      const float gm = rbf(1.f - gate);
      float t0 = rbf(gm * rbf(b0)), t1 = rbf(gm * rbf(b1));
      PIN(t0); PIN(t1);
      for (u32 spin = 0;; ++spin) {
        ld16s(A0, o0, fa); ld16s(A1, o0, fa + 16 * 8192);
        asm volatile("s_waitcnt vmcnt(0)" : "+v"(A0), "+v"(A1) :: "memory");
        const u32 lo = (A0[0] ^ T8) | (A0[2] ^ T8) | (A1[0] ^ T8) | (A1[2] ^ T8);
        const u32 hi = (A0[1] ^ T32) | (A0[3] ^ T32) | (A1[1] ^ T32) | (A1[3] ^ T32);
        if (__builtin_amdgcn_ballot_w64(((lo & 0xFFu) | hi) != 0) == 0 || spin > SPIN_CAP) break;
      }
      float a0 = val(A0[0]) + val(A1[0]), a1 = val(A0[2]) + val(A1[2]);
      a0 += dpp<ROR8>(a0); a1 += dpp<ROR8>(a1);
      a0 += dpp<ROR4>(a0); a1 += dpp<ROR4>(a1);
      a0 = sum32(sum16(a0)); a1 = sum32(sum16(a1));
      const float r0 = rbf(gate * rbf(a0)) + t0, r1 = rbf(gate * rbf(a1)) + t1;
      asm volatile("s_mov_b64 exec, 15\n\tglobal_store_dword %0, %1, %2\n\ts_mov_b64 exec, -1" :: "v"(outoff), "v"(pkbf(r0, r1)), "s"(p.out) : "memory");
      if (bid == 0) asm volatile("s_mov_b64 exec, 1\n\tglobal_store_dword %0, %1, %2 sc1\n\ts_mov_b64 exec, -1" :: "v"(0u), "v"(T32), "s"(p.ws) : "memory");
    }
  }
}

extern "C" void kernel_launch(void* const* d_in, const int* in_sizes, int n_in, void* d_out, int out_size, void* d_ws, size_t ws_size, hipStream_t stream) {
  Params p;
  p.x = (const char*)d_in[0]; p.kc = (const char*)d_in[1]; p.vc = (const char*)d_in[2]; p.wq = (const char*)d_in[3]; p.wo = (const char*)d_in[4];
  p.wg = (const char*)d_in[6]; p.wu = (const char*)d_in[7]; p.wd = (const char*)d_in[8]; p.gw = (const char*)d_in[9];
  p.out = (char*)d_out; p.ws = (char*)d_ws; p.unused = 0;
  fused_decode<<<256, 512, 0, stream>>>(p);
}
```

```cpp
#include <hip/hip_runtime.h>
#include <hip/hip_bf16.h>
#include <stdint.h>

#define LAS __attribute__((address_space(3)))
typedef unsigned u32;
typedef unsigned long long u64;
typedef unsigned u32x4 __attribute__((ext_vector_type(4)));
typedef unsigned u32x2 __attribute__((ext_vector_type(2)));
typedef float f32x4 __attribute__((ext_vector_type(4)));
typedef float f32x2 __attribute__((ext_vector_type(2)));
typedef short s16x4 __attribute__((ext_vector_type(4)));
typedef short s16x8 __attribute__((ext_vector_type(8)));
typedef __bf16 bf16x8 __attribute__((ext_vector_type(8)));
typedef __bf16 bf16x2 __attribute__((ext_vector_type(2)));

#define KV_NT " nt"
#define W_NT " nt"

constexpr u32 WS_EPOCH = 0;
constexpr u32 WS_SQ = 4096;
constexpr u32 WS_SG = WS_SQ + 32 * 8 * 256 * 8;
constexpr u32 WS_SO = WS_SG + 86 * 4 * 128 * 8;
constexpr u32 WS_SF = WS_SO + 32 * 8 * 640;
constexpr u32 WS_SB = WS_SF + 32 * 1024 * 8;
constexpr u32 WS_END = WS_SB + 43 * 1024 * 8;

constexpr u32 L_K = 0, L_WO = 0, L_WD = 16384;
constexpr u32 L_V = 65536, L_WQ = L_V + 32768, L_GU = L_V + 49152;
constexpr u32 L_XS = 131072;
constexpr u32 L_GW = L_XS + 2048;
constexpr u32 L_EP = L_GW + 1024;
constexpr u32 L_ST = L_EP + 1024;
constexpr u32 L_QW = L_ST + 4096;
constexpr u32 L_PM = L_QW + 2048;
constexpr u32 L_SM = L_PM + 8 * 528;
constexpr u32 L_OS = L_SM + 64;
constexpr u32 L_END = L_OS + 8 * 272;
typedef char lds_map_fits[L_END <= 163840 ? 1 : -1];

struct Params {
  const char* x; const char* kc; const char* vc; const char* wq; const char* wo;
  const char* wg; const char* wu; const char* wd; const char* gw; char* out; char* ws; u64 unused;
};
typedef u32 u32x16 __attribute__((ext_vector_type(16)));
typedef u32 u32x8 __attribute__((ext_vector_type(8)));
template <typename T> __device__ __forceinline__ T mkptr(u32 lo, u32 hi) { return (T)(((u64)hi << 32) | lo); }

__device__ __forceinline__ void dma16(u32 voff, const char* sbase, u32 lds) {
  asm volatile("s_mov_b32 m0, %2\n\ts_nop 0\n\tglobal_load_lds_dwordx4 %0, %1" W_NT :: "v"(voff), "s"(sbase), "s"(lds) : "memory");
}
__device__ __forceinline__ void dma16kv(u32 voff, const char* sbase, u32 lds) {
  asm volatile("s_mov_b32 m0, %2\n\ts_nop 0\n\tglobal_load_lds_dwordx4 %0, %1" KV_NT :: "v"(voff), "s"(sbase), "s"(lds) : "memory");
}
__device__ __forceinline__ void dma4kv(u32 v0, u32 v1, u32 v2, u32 v3, const char* sbase, u32 lds) {
  asm volatile("s_mov_b32 m0, %5\n\ts_nop 0\n\tglobal_load_lds_dwordx4 %0, %4" KV_NT "\n\tglobal_load_lds_dwordx4 %1, %4 offset:1024" KV_NT
               "\n\tglobal_load_lds_dwordx4 %2, %4 offset:2048" KV_NT "\n\tglobal_load_lds_dwordx4 %3, %4 offset:3072" KV_NT
               :: "v"(v0), "v"(v1), "v"(v2), "v"(v3), "s"(sbase), "s"(lds) : "memory");
}
__device__ __forceinline__ void dma16p(u32 voff, const char* sbase, u32 lds) {
  asm volatile("s_mov_b32 m0, %2\n\ts_nop 0\n\tglobal_load_lds_dwordx4 %0, %1 sc1" :: "v"(voff), "s"(sbase), "s"(lds) : "memory");
}
__device__ __forceinline__ void ld16s(u32x4& d, u32 voff, const char* sbase) {
  asm volatile("global_load_dwordx4 %0, %1, %2 sc1" : "=v"(d) : "v"(voff), "s"(sbase) : "memory");
}
__device__ __forceinline__ void st8s(u32 voff, u32x2 d, char* sbase) {
  asm volatile("global_store_dwordx2 %0, %1, %2 sc1" :: "v"(voff), "v"(d), "s"(sbase) : "memory");
}
#define WAITVM(n) asm volatile("s_waitcnt vmcnt(" #n ")" ::: "memory")
#define BARRIER() asm volatile("s_waitcnt lgkmcnt(0)\n\ts_barrier" ::: "memory")
#define PIN(x) asm volatile("" : "+v"(x))

template <typename T> __device__ __forceinline__ T lds_ld(const LAS char* base, u32 off) { return *(const LAS T*)(base + off); }
template <typename T> __device__ __forceinline__ void lds_st(LAS char* base, u32 off, T v) { *(LAS T*)(base + off) = v; }
__device__ __forceinline__ s16x4 trd(const LAS char* base, u32 off) {
  return __builtin_bit_cast(s16x4, __builtin_amdgcn_ds_read_tr16_b64_v4i16((LAS s16x4*)(base + off)));
}
__device__ __forceinline__ bf16x8 cat8(s16x4 a, s16x4 b) {
  s16x8 r = __builtin_shufflevector(a, b, 0, 1, 2, 3, 4, 5, 6, 7); return __builtin_bit_cast(bf16x8, r);
}
__device__ __forceinline__ f32x4 mfma(bf16x8 a, bf16x8 b, f32x4 c) { return __builtin_amdgcn_mfma_f32_16x16x32_bf16(a, b, c, 0, 0, 0); }
__device__ __forceinline__ u32 pkbf(float lo, float hi) {
  f32x2 v = {lo, hi}; return __builtin_bit_cast(u32, __builtin_convertvector(v, bf16x2));
}
__device__ __forceinline__ float rbf(float v) { return __uint_as_float(pkbf(v, v) << 16); }
template <int C> __device__ __forceinline__ float dpp(float v) {
  return __builtin_bit_cast(float, __builtin_amdgcn_mov_dpp(__builtin_bit_cast(int, v), C, 0xf, 0xf, true));
}
constexpr int ROR1 = 0x121, ROR2 = 0x122, ROR4 = 0x124, ROR8 = 0x128, QX1 = 0xB1, QX2 = 0x4E;
__device__ __forceinline__ float sum16(float v) { auto s = __builtin_amdgcn_permlane16_swap(__float_as_uint(v), __float_as_uint(v), false, false); return __uint_as_float(s[0]) + __uint_as_float(s[1]); }
__device__ __forceinline__ float sum32(float v) { auto s = __builtin_amdgcn_permlane32_swap(__float_as_uint(v), __float_as_uint(v), false, false); return __uint_as_float(s[0]) + __uint_as_float(s[1]); }
__device__ __forceinline__ float max16(float v) { auto s = __builtin_amdgcn_permlane16_swap(__float_as_uint(v), __float_as_uint(v), false, false); return fmaxf(__uint_as_float(s[0]), __uint_as_float(s[1])); }
__device__ __forceinline__ float max32(float v) { auto s = __builtin_amdgcn_permlane32_swap(__float_as_uint(v), __float_as_uint(v), false, false); return fmaxf(__uint_as_float(s[0]), __uint_as_float(s[1])); }
__device__ __forceinline__ float ex2(float v) { return __builtin_amdgcn_exp2f(v); }
constexpr float LOG2E = 1.4426950408889634f;
constexpr u32 SPIN_CAP = 1u << 16;

#define GCHECK "v_xor_b32 %[t0], %[T8], %[q0]\n\tv_xor_b32 %[t1], %[T8], %[q2]\n\tv_or_b32 %[t0], %[t0], %[t1]\n\tv_and_b32 %[t0], 0xff, %[t0]\n\t" \
               "v_xor_b32 %[t1], %[T32], %[q1]\n\tv_or_b32 %[t0], %[t0], %[t1]\n\tv_xor_b32 %[t1], %[T32], %[q3]\n\tv_or_b32 %[t0], %[t0], %[t1]\n\t" \
               "v_cmp_ne_u32 vcc, 0, %[t0]\n\t"

__global__ void __launch_bounds__(512, 2) fused_decode(Params unused_) {
  Params p;
  {
    u32x16 A; u32x8 B;
    asm volatile("s_load_dwordx16 %0, %2, 0x0\n\ts_load_dwordx8 %1, %2, 0x40\n\ts_waitcnt lgkmcnt(0)" : "=&s"(A), "=&s"(B) : "s"(__builtin_amdgcn_kernarg_segment_ptr()));
    p.x = mkptr<const char*>(A[0], A[1]); p.kc = mkptr<const char*>(A[2], A[3]); p.vc = mkptr<const char*>(A[4], A[5]); p.wq = mkptr<const char*>(A[6], A[7]);
    p.wo = mkptr<const char*>(A[8], A[9]); p.wg = mkptr<const char*>(A[10], A[11]); p.wu = mkptr<const char*>(A[12], A[13]); p.wd = mkptr<const char*>(A[14], A[15]);
    p.gw = mkptr<const char*>(B[0], B[1]); p.out = mkptr<char*>(B[2], B[3]); p.ws = mkptr<char*>(B[4], B[5]);
  }
  __shared__ __attribute__((aligned(1024))) char smem_[L_END];
  LAS char* sm = (LAS char*)smem_;
  const u32 tid = threadIdx.x, lane = tid & 63;
  const u32 w = __builtin_amdgcn_readfirstlane(tid >> 6);
  const u32 bid = blockIdx.x;
  const u32 h = bid >> 3, sub = (bid + h) & 7, b = sub >> 2, s = sub & 3;
  const bool roleG = bid < 172, roleD = bid >= 170;
  const u32 g = lane >> 4, col = lane & 15, r4 = (lane >> 2) & 3, c4 = lane & 3;
  const u32 hf = (lane & 1) << 4, un = (lane & 15) >> 1;
  {
    const u32 vo = g * 8192 + ((un ^ (g | ((w & 1) << 2))) << 5) + hf;
    const char* base = p.wq + (size_t)(64 * sub + 8 * w) * 8192 + h * 256;
    dma16(vo, base, L_WQ + w * 2048);
    dma16(vo + 4 * 8192, base, L_WQ + w * 2048 + 1024);
  }
  if (w < 4) dma16p(lane * 16, w == 0 ? p.x : w == 1 ? p.x + 1024 : w == 2 ? p.gw : p.ws + WS_EPOCH, L_XS + w * 1024);
  const u32 vo128 = ((((lane & 7) >> 1) ^ ((lane >> 4) & 1)) << 5) + hf;
  if (roleG) {
    const u32 ell = bid >> 2, rg = bid & 3;
    const u32 vo = (lane >> 3) * 5504 + (vo128 ^ ((w & 1) << 6));
    const u32 o = (64 * rg + 8 * w) * 5504 + ell * 128;
    dma16(vo, p.wg + o, L_GU + w * 1024);
    dma16(vo, p.wu + o, L_GU + 8192 + w * 1024);
  }
  const size_t kvrow = ((size_t)(b * 32 + h) * 4096 + 3072 + 256 * s) * 256;
  const u32 vok = g * 256 + ((col ^ g) << 4);
  const char* kbase = p.kc + kvrow + (size_t)w * 8192;
  dma4kv(vok, vok ^ 64, vok ^ 128, vok ^ 192, kbase, L_K + w * 8192);
  lds_st<u32x2>(sm, L_PM + w * 528 + lane * 8, u32x2{0u, 0u});
  if (roleG) WAITVM(6); else WAITVM(4);
  BARRIER();
  const u32 T32 = __builtin_amdgcn_readfirstlane(lds_ld<u32>(sm, L_EP)) + 1u;
  const u32 T8 = (T32 ^ 0x5Au) & 0xFFu;
  {
    const u32 xa = L_XS + (col & 1) * 1024 + (64 * sub + 8 * g) * 2;
    const u32 tb = L_WQ + (8 * g + r4) * 256 + ((w ^ (r4 | ((g & 1) << 2))) << 5) + c4 * 8;
    f32x4 acc = {0.f, 0.f, 0.f, 0.f};
#pragma unroll
    for (int t = 0; t < 2; ++t) {
      const bf16x8 a = lds_ld<bf16x8>(sm, xa + t * 64);
      const bf16x8 bb = cat8(trd(sm, tb + t * 8192), trd(sm, tb + t * 8192 + 1024));
      acc = mfma(a, bb, acc);
    }
    const float v = (g & 1) ? acc[1] : acc[0];
    if (g < 2) {
      u32x2 d = {(__float_as_uint(v) & 0xFFFFFF00u) | T8, T32};
      st8s((g * 128 + 16 * w + col) * 8, d, p.ws + WS_SQ + (size_t)(h * 8 + sub) * 2048);
    }
  }
  if (roleG) {
    WAITVM(5);
    BARRIER();
    const u32 ell = bid >> 2, rg = bid & 3;
    const u32 hl = 2 * ell + ((w & 3) >> 1);
    const u32 sel = hl >= 43 ? 0x07060302u : 0x05040100u;
    const u32 xa = L_XS + (col & 1) * 1024 + (128 * rg + 16 * g) * 2;
    const u32 tb = L_GU + (w >> 2) * 8192 + (8 * g + r4) * 128 + (((w & 3) ^ ((r4 >> 1) | ((g & 1) << 1))) << 5) + c4 * 8;
    f32x4 acc = {0.f, 0.f, 0.f, 0.f};
#pragma unroll
    for (int t = 0; t < 2; ++t) {
      const u32x4 x0 = lds_ld<u32x4>(sm, xa + t * 128), x1 = lds_ld<u32x4>(sm, xa + t * 128 + 16);
      const u32x4 a = {__builtin_amdgcn_perm(x0[1], x0[0], sel), __builtin_amdgcn_perm(x0[3], x0[2], sel),
                       __builtin_amdgcn_perm(x1[1], x1[0], sel), __builtin_amdgcn_perm(x1[3], x1[2], sel)};
      const bf16x8 bb = cat8(trd(sm, tb + t * 4096), trd(sm, tb + t * 4096 + 512));
      acc = mfma(__builtin_bit_cast(bf16x8, a), bb, acc);
    }
    const float v = (g & 1) ? acc[1] : acc[0];
    if (g < 2) {
      u32x2 d = {(__float_as_uint(v) & 0xFFFFFF00u) | T8, T32};
      st8s((((w >> 2) * 2 + g) * 32 + 16 * (w & 1) + col) * 8, d, p.ws + WS_SG + (size_t)(hl * 4 + rg) * 1024);
    }
  }
  dma4kv(vok, vok ^ 64, vok ^ 128, vok ^ 192, kbase + 4096, L_K + w * 8192 + 4096);
  const char* qsrc = p.ws + WS_SQ + (size_t)(h * 8 + w) * 2048 + b * 1024;
  asm volatile("global_load_dwordx4 v[104:107], %0, %1 sc1" :: "v"(lane * 16), "s"(qsrc) : "memory", "v104", "v105", "v106", "v107");
  const u32 vov = g * 256 + ((((lane & 15) >> 1) ^ (g | (((w >> 1) & 1) << 2))) << 5) + ((lane & 1) << 4);
  const char* vbase = p.vc + kvrow + (size_t)w * 1024;
#pragma unroll
  for (int i = 0; i < 4; ++i) dma16kv(vov, vbase + i * 8192, L_V + w * 1024 + i * 8192);
  float gate = 0.f;
  const bool fin = bid < 128 && w == 4;
  if (fin) {
    const u32x4 xv = lds_ld<u32x4>(sm, L_XS + (bid >> 6) * 1024 + lane * 16), gv = lds_ld<u32x4>(sm, L_GW + lane * 16);
    float a = 0.f;
#pragma unroll
    for (int k = 0; k < 4; ++k) {
      a += rbf(__uint_as_float(xv[k] << 16) * __uint_as_float(gv[k] << 16));
      a += rbf(__uint_as_float(xv[k] & 0xFFFF0000u) * __uint_as_float(gv[k] & 0xFFFF0000u));
    }
    a += dpp<ROR8>(a); a += dpp<ROR4>(a); a += dpp<ROR2>(a); a += dpp<ROR1>(a);
    a = sum32(sum16(a));
    a = rbf(a);
    gate = rbf(1.f / (1.f + __expf(-a)));
  }
  PIN(gate);
  {
    u32 q0, q1, q2, q3, t0, t1, cnt;
    asm volatile(
        "s_waitcnt vmcnt(4)\n\t"
        "s_mov_b32 %[cnt], 0\n"
        "1:\n\t"
        "v_mov_b32 %[q0], v104\n\tv_mov_b32 %[q1], v105\n\tv_mov_b32 %[q2], v106\n\tv_mov_b32 %[q3], v107\n\t"
        GCHECK
        "s_cbranch_vccz 9f\n\t"
        "s_add_u32 %[cnt], %[cnt], 1\n\ts_cmp_gt_u32 %[cnt], 0x10000\n\ts_cbranch_scc1 9f\n\t"
        "global_load_dwordx4 v[104:107], %[off], %[src] sc1\n\t"
        "s_waitcnt vmcnt(0)\n\t"
        "s_branch 1b\n"
        "9:\n\t"
        : [q0] "=&v"(q0), [q1] "=&v"(q1), [q2] "=&v"(q2), [q3] "=&v"(q3), [t0] "=&v"(t0), [t1] "=&v"(t1), [cnt] "=&s"(cnt)
        : [off] "v"(lane * 16), [src] "s"(qsrc), [T8] "s"(T8), [T32] "s"(T32)
        : "memory", "scc", "vcc", "v104", "v105", "v106", "v107");
    lds_st<u32x2>(sm, L_ST + w * 512 + lane * 8, u32x2{q0 & 0xFFFFFF00u, q2 & 0xFFFFFF00u});
  }
  BARRIER();
#pragma unroll
  for (int i = 4; i < 8; ++i) dma16kv(vov, vbase + i * 8192, L_V + w * 1024 + i * 8192);
  {
    f32x2 q2 = lds_ld<f32x2>(sm, L_ST + lane * 8);
#pragma unroll
    for (int j = 1; j < 8; ++j) q2 += lds_ld<f32x2>(sm, L_ST + j * 512 + lane * 8);
    lds_st<u32>(sm, L_QW + w * 256 + lane * 4, pkbf(q2[0], q2[1]));
    asm volatile("s_waitcnt lgkmcnt(0)" ::: "memory");
    const u32 kb = L_K + (32 * w + col) * 256 + ((g ^ col) << 4);
    f32x4 a0 = {0.f, 0.f, 0.f, 0.f}, a1 = a0;
#pragma unroll
    for (int t = 0; t < 4; ++t) {
      const bf16x8 aq = lds_ld<bf16x8>(sm, L_QW + w * 256 + t * 64 + g * 16);
      a0 = mfma(aq, lds_ld<bf16x8>(sm, kb ^ (t << 6)), a0);
      a1 = mfma(aq, lds_ld<bf16x8>(sm, (kb ^ (t << 6)) + 4096), a1);
    }
    const float s0 = (g & 2) ? a1[0] : a0[0];
    const float s2 = rbf(rbf(s0) * 0.08838834764831845f);
    float m = s2;
    m = fmaxf(m, dpp<ROR8>(m)); m = fmaxf(m, dpp<ROR4>(m)); m = fmaxf(m, dpp<ROR2>(m)); m = fmaxf(m, dpp<ROR1>(m));
    m = max32(m);
    const float e = ex2((s2 - m) * LOG2E);
    float l = e;
    l += dpp<ROR8>(l); l += dpp<ROR4>(l); l += dpp<ROR2>(l); l += dpp<ROR1>(l);
    l = sum32(l);
    if ((g & 1) == 0) lds_st<unsigned short>(sm, L_PM + w * 528 + (32 * w + 16 * (g >> 1) + col) * 2, (unsigned short)pkbf(e, e));
    if (lane < 2) lds_st<float>(sm, L_SM + lane * 32 + w * 4, lane ? l : m);
  }
  BARRIER();
  {
    const u32 vo = (lane >> 3) * 1024 + vo128;
    const char* base = p.wo + (size_t)(h * 128 + 16 * w) * 1024 + sub * 128;
    dma16(vo, base, L_WO + (16 * w) * 128);
    dma16(vo ^ 64, base + 8 * 1024, L_WO + (16 * w + 8) * 128);
  }
  const u32 rd = (bid - 170) >> 1, cd = (bid - 170) & 1;
  const char* gsrc = p.ws + WS_SG + (size_t)(((w >> 2) * 43 + rd) * 4 + (w & 3)) * 1024;
  if (roleD) {
    asm volatile("global_load_dwordx4 v[108:111], %0, %1 sc1" :: "v"(lane * 16), "s"(gsrc) : "memory", "v108", "v109", "v110", "v111");
    const u32 f0 = (lane >> 5) | (((w >> 1) & 1) << 2);
    const u32 vo = (lane >> 5) * 1024 + ((((lane & 31) >> 1) ^ f0) << 5) + ((lane & 1) << 4);
    const char* base = p.wd + (size_t)(32 * rd + 4 * w) * 1024 + cd * 512;
    dma16(vo, base, L_WD + (4 * w) * 512);
    dma16(vo ^ 64, base + 2 * 1024, L_WD + (4 * w + 2) * 512);
  }
  float cw, Lw, Mw;
  {
    const u32 j4 = (((g & 1) << 2) | c4) * 4;
    const float mj = lds_ld<float>(sm, L_SM + j4), lj = lds_ld<float>(sm, L_SM + 32 + j4);
    float M = fmaxf(mj, dpp<QX1>(mj)); M = fmaxf(M, dpp<QX2>(M));
    Mw = max16(M);
    cw = ex2((mj - Mw) * LOG2E);
    float lp = cw * lj; lp += dpp<QX1>(lp); lp += dpp<QX2>(lp);
    Lw = sum16(lp);
  }
  bf16x8 ap[8];
#pragma unroll
  for (int t = 0; t < 8; ++t) ap[t] = lds_ld<bf16x8>(sm, L_PM + (col & 7) * 528 + t * 64 + g * 16);
  const u32 vb = L_V + (8 * g + r4) * 256 + ((w ^ (r4 | ((g & 1) << 2))) << 5) + c4 * 8;
  u32 ooff = lane < 16 ? (8 * w + (col >> 1)) * 8 : 512 + (lane - 16) * 8;
  u32 ostat = (__float_as_uint(lane == 16 ? Mw : Lw) & 0xFFFFFF00u) | T8;
  const u64 omask = 0x5555ull | (w == 0 ? 0x30000ull : 0ull);
  char* odst = p.ws + WS_SO + (size_t)(h * 8 + sub) * 640;
  float cw0 = dpp<0x00>(cw), cw1 = dpp<0x55>(cw), cw2 = dpp<0xAA>(cw), cw3 = dpp<0xFF>(cw);
  PIN(ooff); PIN(ostat); PIN(cw0); PIN(cw1); PIN(cw2); PIN(cw3);
  f32x4 oa = {0.f, 0.f, 0.f, 0.f};
  if (roleD) WAITVM(7); else WAITVM(4);
  BARRIER();
#pragma unroll
  for (int t = 0; t < 6; ++t) oa = mfma(ap[t], cat8(trd(sm, vb + t * 8192), trd(sm, vb + t * 8192 + 1024)), oa);
  PIN(oa);
  if (roleD) WAITVM(5); else WAITVM(2);
  BARRIER();
#pragma unroll
  for (int t = 6; t < 8; ++t) oa = mfma(ap[t], cat8(trd(sm, vb + t * 8192), trd(sm, vb + t * 8192 + 1024)), oa);
  {
    float v = oa[0] * cw0;
    v = fmaf(oa[1], cw1, v); v = fmaf(oa[2], cw2, v); v = fmaf(oa[3], cw3, v);
    v = sum16(v);
    const u32 pr = pkbf(v, dpp<QX1>(v));
    u32x2 d = {lane < 16 ? pr : ostat, T32};
    asm volatile("s_mov_b64 exec, %3\n\tglobal_store_dwordx2 %0, %1, %2 sc1\n\ts_mov_b64 exec, -1" :: "v"(ooff), "v"(d), "s"(odst), "s"(omask) : "memory");
  }
  WAITVM(1);
  if (roleD) {
    u32 q0, q1, q2, q3, t0, t1, cnt;
    asm volatile(
        "s_mov_b32 %[cnt], 0\n"
        "1:\n\t"
        "v_mov_b32 %[q0], v108\n\tv_mov_b32 %[q1], v109\n\tv_mov_b32 %[q2], v110\n\tv_mov_b32 %[q3], v111\n\t"
        GCHECK
        "s_cbranch_vccz 9f\n\t"
        "s_add_u32 %[cnt], %[cnt], 1\n\ts_cmp_gt_u32 %[cnt], 0x10000\n\ts_cbranch_scc1 9f\n\t"
        "global_load_dwordx4 v[108:111], %[off], %[src] sc1\n\t"
        "s_waitcnt vmcnt(0)\n\t"
        "s_branch 1b\n"
        "9:\n\t"
        : [q0] "=&v"(q0), [q1] "=&v"(q1), [q2] "=&v"(q2), [q3] "=&v"(q3), [t0] "=&v"(t0), [t1] "=&v"(t1), [cnt] "=&s"(cnt)
        : [off] "v"(lane * 16), [src] "s"(gsrc), [T8] "s"(T8), [T32] "s"(T32)
        : "memory", "scc", "vcc", "v108", "v109", "v110", "v111");
    lds_st<u32x2>(sm, L_ST + w * 512 + lane * 8, u32x2{q0 & 0xFFFFFF00u, q2 & 0xFFFFFF00u});
  }
  u32 wdt = L_WD + (8 * g + r4) * 512 + (((4 * (w & 3)) ^ (r4 | ((g & 1) << 2))) << 5) + c4 * 8;
  u32 hwr = L_QW + w * 256 + lane * 4, hrd = L_QW + w * 256 + (col & 1) * 64 + g * 16;
  u32 boff = ((g & 1) * 512 + 256 * cd + 16 * (4 * (w & 3) + (g >> 1)) + col) * 8;
  char* bdst = p.ws + WS_SB + (size_t)rd * 8192;
  PIN(wdt); PIN(hwr); PIN(hrd); PIN(boff);
  BARRIER();
  if (w < 4) __builtin_amdgcn_s_setprio(2);
  const char* osrc = p.ws + WS_SO + (size_t)(h * 8) * 640;
  const u32 soff = (((g & 1) << 2) | c4) * 640 + 512;
  bf16x8 wb[4];
  if (w < 4) {
    const u32 tb = L_WO + (8 * g + r4) * 128 + ((w ^ ((r4 >> 1) | ((g & 1) << 1))) << 5) + c4 * 8;
#pragma unroll
    for (int t = 0; t < 4; ++t) wb[t] = cat8(trd(sm, tb + t * 4096), trd(sm, tb + t * 4096 + 512));
  } else {
    if (roleD) {
      bf16x8 wf[4];
#pragma unroll
      for (int q = 0; q < 4; ++q) wf[q] = cat8(trd(sm, wdt ^ (q << 5)), trd(sm, (wdt ^ (q << 5)) + 2048));
      f32x2 gs[8];
#pragma unroll
      for (int j = 0; j < 8; ++j) gs[j] = lds_ld<f32x2>(sm, L_ST + j * 512 + lane * 8);
      __builtin_amdgcn_sched_barrier(0);
      f32x2 v2 = gs[0];
#pragma unroll
      for (int j = 1; j < 8; ++j) v2 += gs[j];
      const u32 pr = pkbf(v2[0], v2[1]);
      auto sw = __builtin_amdgcn_permlane32_swap(pr, pr, false, false);
      const u32 gq = sw[0], uq = sw[1];
      const u32 hp = pkbf(__uint_as_float(gq << 16) * __uint_as_float(uq << 16), __uint_as_float(gq & 0xFFFF0000u) * __uint_as_float(uq & 0xFFFF0000u));
      lds_st<u32>(sm, hwr, hp);
      asm volatile("s_waitcnt lgkmcnt(0)" ::: "memory");
      const bf16x8 ah = lds_ld<bf16x8>(sm, hrd);
      const f32x4 z = {0.f, 0.f, 0.f, 0.f};
      f32x4 acc[4];
#pragma unroll
      for (int q = 0; q < 4; ++q) acc[q] = mfma(ah, wf[q], z);
      asm volatile("" : "+v"(acc[0]), "+v"(acc[1]), "+v"(acc[2]), "+v"(acc[3]) : "v"(wf[0]), "v"(wf[1]), "v"(wf[2]), "v"(wf[3]));
#pragma unroll
      for (int qq = 0; qq < 2; ++qq) {
        const f32x4 x0 = acc[2 * qq], x1 = acc[2 * qq + 1];
        const float lo = (g & 1) ? x0[1] : x0[0], hi = (g & 1) ? x1[1] : x1[0];
        const float v = (g & 2) ? hi : lo;
        u32x2 d = {(__float_as_uint(v) & 0xFFFFFF00u) | T8, T32};
        st8s(boff + qq * 256, d, bdst);
      }
    }
  }
  u32 foff = ((g & 1) * 512 + 64 * sub + 16 * w + col) * 8, aoff = L_OS + (col & 7) * 272 + g * 16;
  char* fdst = p.ws + WS_SF + (size_t)h * 8192;
  PIN(foff); PIN(aoff);
  u32 sM, sL;
  {
    u32 t0, t1, cnt;
    u32 ooffr = w * 640 + lane * 8, osta = L_OS + w * 272 + lane * 4;
    asm volatile(
        "s_mov_b32 %[cnt], 0\n"
        "1:\n\t"
        "global_load_dwordx2 v[104:105], %[oo], %[src] sc1\n\t"
        "global_load_dwordx4 v[108:111], %[so], %[src] sc1\n\t"
        "s_waitcnt vmcnt(0)\n\t"
        "v_xor_b32 %[t0], %[T8], v108\n\tv_xor_b32 %[t1], %[T8], v110\n\tv_or_b32 %[t0], %[t0], %[t1]\n\tv_and_b32 %[t0], 0xff, %[t0]\n\t"
        "v_xor_b32 %[t1], %[T32], v105\n\tv_or_b32 %[t0], %[t0], %[t1]\n\tv_xor_b32 %[t1], %[T32], v109\n\tv_or_b32 %[t0], %[t0], %[t1]\n\t"
        "v_xor_b32 %[t1], %[T32], v111\n\tv_or_b32 %[t0], %[t0], %[t1]\n\t"
        "v_cmp_ne_u32 vcc, 0, %[t0]\n\ts_cbranch_vccz 9f\n\t"
        "s_add_u32 %[cnt], %[cnt], 1\n\ts_cmp_lt_u32 %[cnt], 0x10000\n\ts_cbranch_scc1 1b\n"
        "9:\n\t"
        "ds_write_b32 %[osta], v104\n\t"
        "v_and_b32 %[sM], 0xffffff00, v108\n\tv_and_b32 %[sL], 0xffffff00, v110\n\ts_nop 1\n\t"
        : [sM] "=&v"(sM), [sL] "=&v"(sL), [t0] "=&v"(t0), [t1] "=&v"(t1), [cnt] "=&s"(cnt)
        : [oo] "v"(ooffr), [so] "v"(soff), [src] "s"(osrc), [osta] "v"(osta), [T8] "s"(T8), [T32] "s"(T32)
        : "memory", "scc", "vcc", "v104", "v105", "v108", "v109", "v110", "v111");
  }
  BARRIER();
  if (w < 4) {
    bf16x8 a0 = lds_ld<bf16x8>(sm, aoff), a1 = lds_ld<bf16x8>(sm, aoff + 64), a2 = lds_ld<bf16x8>(sm, aoff + 128), a3 = lds_ld<bf16x8>(sm, aoff + 192);
    __builtin_amdgcn_sched_barrier(0);
    float c;
    {
      const float Mj = __uint_as_float(sM), Lj = __uint_as_float(sL);
      float Mb = fmaxf(Mj, dpp<QX1>(Mj)); Mb = fmaxf(Mb, dpp<QX2>(Mb));
      const float e = ex2((Mj - Mb) * LOG2E);
      float Lb = e * Lj; Lb += dpp<QX1>(Lb); Lb += dpp<QX2>(Lb);
      c = e * __builtin_amdgcn_rcpf(Lb);
    }
    float c0 = dpp<0x00>(c), c1 = dpp<0x55>(c), c2 = dpp<0xAA>(c), c3 = dpp<0xFF>(c);
    asm volatile("" : "+v"(a0), "+v"(a1), "+v"(a2), "+v"(a3), "+v"(c0), "+v"(c1), "+v"(c2), "+v"(c3));
    const f32x4 z = {0.f, 0.f, 0.f, 0.f};
    f32x4 acc = mfma(a0, wb[0], z), acc2 = mfma(a2, wb[2], z);
    acc = mfma(a1, wb[1], acc); acc2 = mfma(a3, wb[3], acc2);
    acc += acc2;
    float v = acc[0] * c0;
    v = fmaf(acc[1], c1, v); v = fmaf(acc[2], c2, v); v = fmaf(acc[3], c3, v);
    const u32x2 d = {(__float_as_uint(v) & 0xFFFFFF00u) | T8, T32};
    asm volatile("s_mov_b64 exec, %3\n\tglobal_store_dwordx2 %0, %1, %2 sc1\n\ts_mov_b64 exec, -1" :: "v"(foff), "v"(d), "s"(fdst), "s"(0xFFFFFFFFull) : "memory");
  } else {
    if (fin) {
      const u32 fb = bid >> 6, n0 = (bid & 63) * 8;
      const u32 hh = lane >> 2;
      const u32 o0 = (fb * 512 + n0 + 2 * c4) * 8 + hh * 8192;
      const u32 hh2 = hh < 11 ? hh : 0u;
      const u32 o2 = (fb * 512 + n0 + 2 * c4) * 8 + (32 + hh2) * 8192;
      const char* fa = p.ws + WS_SF; const char* fbp = p.ws + WS_SB;
      u32 outoff = (fb * 512 + n0 + 2 * lane) * 2;
      PIN(outoff);
      u32x4 A0, A1, B0, B1, B2;
      for (u32 spin = 0;; ++spin) {
        ld16s(B0, o0, fbp); ld16s(B1, o0, fbp + 16 * 8192); ld16s(B2, o2, fbp);
        asm volatile("s_waitcnt vmcnt(0)" : "+v"(B0), "+v"(B1), "+v"(B2) :: "memory");
        const u32 lo = (B0[0] ^ T8) | (B0[2] ^ T8) | (B1[0] ^ T8) | (B1[2] ^ T8) | (B2[0] ^ T8) | (B2[2] ^ T8);
        const u32 hi = (B0[1] ^ T32) | (B0[3] ^ T32) | (B1[1] ^ T32) | (B1[3] ^ T32) | (B2[1] ^ T32) | (B2[3] ^ T32);
        if (__builtin_amdgcn_ballot_w64(((lo & 0xFFu) | hi) != 0) == 0 || spin > SPIN_CAP) break;
      }
      auto val = [](u32 v) { return __uint_as_float(v & 0xFFFFFF00u); };
      float b0 = val(B0[0]) + val(B1[0]), b1 = val(B0[2]) + val(B1[2]);
      if (hh < 11) { b0 += val(B2[0]); b1 += val(B2[2]); }
      b0 += dpp<ROR8>(b0); b1 += dpp<ROR8>(b1);
      b0 += dpp<ROR4>(b0); b1 += dpp<ROR4>(b1);
      b0 = sum32(sum16(b0)); b1 = sum32(sum16(b1));
      const float gm = rbf(1.f - gate);
      float t0 = rbf(gm * rbf(b0)), t1 = rbf(gm * rbf(b1));
      PIN(t0); PIN(t1);
      for (u32 spin = 0;; ++spin) {
        ld16s(A0, o0, fa); ld16s(A1, o0, fa + 16 * 8192);
        asm volatile("s_waitcnt vmcnt(0)" : "+v"(A0), "+v"(A1) :: "memory");
        const u32 lo = (A0[0] ^ T8) | (A0[2] ^ T8) | (A1[0] ^ T8) | (A1[2] ^ T8);
        const u32 hi = (A0[1] ^ T32) | (A0[3] ^ T32) | (A1[1] ^ T32) | (A1[3] ^ T32);
        if (__builtin_amdgcn_ballot_w64(((lo & 0xFFu) | hi) != 0) == 0 || spin > SPIN_CAP) break;
      }
      float a0 = val(A0[0]) + val(A1[0]), a1 = val(A0[2]) + val(A1[2]);
      a0 += dpp<ROR8>(a0); a1 += dpp<ROR8>(a1);
      a0 += dpp<ROR4>(a0); a1 += dpp<ROR4>(a1);
      a0 = sum32(sum16(a0)); a1 = sum32(sum16(a1));
      const float r0 = rbf(gate * rbf(a0)) + t0, r1 = rbf(gate * rbf(a1)) + t1;
      asm volatile("s_mov_b64 exec, 15\n\tglobal_store_dword %0, %1, %2\n\ts_mov_b64 exec, -1" :: "v"(outoff), "v"(pkbf(r0, r1)), "s"(p.out) : "memory");
      if (bid == 0) asm volatile("s_mov_b64 exec, 1\n\tglobal_store_dword %0, %1, %2 sc1\n\ts_mov_b64 exec, -1" :: "v"(0u), "v"(T32), "s"(p.ws) : "memory");
    }
  }
}

extern "C" void kernel_launch(void* const* d_in, const int* in_sizes, int n_in, void* d_out, int out_size, void* d_ws, size_t ws_size, hipStream_t stream) {
  Params p;
  p.x = (const char*)d_in[0]; p.kc = (const char*)d_in[1]; p.vc = (const char*)d_in[2]; p.wq = (const char*)d_in[3]; p.wo = (const char*)d_in[4];
  p.wg = (const char*)d_in[6]; p.wu = (const char*)d_in[7]; p.wd = (const char*)d_in[8]; p.gw = (const char*)d_in[9];
  p.out = (char*)d_out; p.ws = (char*)d_ws; p.unused = 0;
  fused_decode<<<256, 512, 0, stream>>>(p);
}
```

```cpp
#include <hip/hip_runtime.h>
#include <hip/hip_bf16.h>
#include <stdint.h>

#define LAS __attribute__((address_space(3)))
typedef unsigned u32;
typedef unsigned long long u64;
typedef unsigned u32x4 __attribute__((ext_vector_type(4)));
typedef unsigned u32x2 __attribute__((ext_vector_type(2)));
typedef float f32x4 __attribute__((ext_vector_type(4)));
typedef float f32x2 __attribute__((ext_vector_type(2)));
typedef short s16x4 __attribute__((ext_vector_type(4)));
typedef short s16x8 __attribute__((ext_vector_type(8)));
typedef __bf16 bf16x8 __attribute__((ext_vector_type(8)));
typedef __bf16 bf16x2 __attribute__((ext_vector_type(2)));

#define KV_NT " nt"
#define W_NT " nt"

constexpr u32 WS_EPOCH = 0;
constexpr u32 WS_SQ = 4096;
constexpr u32 WS_SG = WS_SQ + 32 * 8 * 256 * 8;
constexpr u32 WS_SO = WS_SG + 86 * 4 * 128 * 8;
constexpr u32 WS_SF = WS_SO + 32 * 8 * 640;
constexpr u32 WS_SB = WS_SF + 32 * 1024 * 8;
constexpr u32 WS_END = WS_SB + 43 * 1024 * 8;

constexpr u32 L_K = 0, L_WO = 0, L_WD = 16384;
constexpr u32 L_V = 65536, L_WQ = L_V + 32768, L_GU = L_V + 49152;
constexpr u32 L_XS = 131072;
constexpr u32 L_GW = L_XS + 2048;
constexpr u32 L_EP = L_GW + 1024;
constexpr u32 L_ST = L_EP + 1024;
constexpr u32 L_QW = L_ST + 4096;
constexpr u32 L_PM = L_QW + 2048;
constexpr u32 L_SM = L_PM + 8 * 528;
constexpr u32 L_OS = L_SM + 64;
constexpr u32 L_END = L_OS + 8 * 272;
typedef char lds_map_fits[L_END <= 163840 ? 1 : -1];

struct Params {
  const char* x; const char* kc; const char* vc; const char* wq; const char* wo;
  const char* wg; const char* wu; const char* wd; const char* gw; char* out; char* ws; u64 unused;
};
typedef u32 u32x16 __attribute__((ext_vector_type(16)));
typedef u32 u32x8 __attribute__((ext_vector_type(8)));
template <typename T> __device__ __forceinline__ T mkptr(u32 lo, u32 hi) { return (T)(((u64)hi << 32) | lo); }

__device__ __forceinline__ void dma16(u32 voff, const char* sbase, u32 lds) {
  asm volatile("s_mov_b32 m0, %2\n\ts_nop 0\n\tglobal_load_lds_dwordx4 %0, %1" W_NT :: "v"(voff), "s"(sbase), "s"(lds) : "memory");
}
__device__ __forceinline__ void dma16kv(u32 voff, const char* sbase, u32 lds) {
  asm volatile("s_mov_b32 m0, %2\n\ts_nop 0\n\tglobal_load_lds_dwordx4 %0, %1" KV_NT :: "v"(voff), "s"(sbase), "s"(lds) : "memory");
}
__device__ __forceinline__ void dma4kv(u32 v0, u32 v1, u32 v2, u32 v3, const char* sbase, u32 lds) {
  asm volatile("s_mov_b32 m0, %5\n\ts_nop 0\n\tglobal_load_lds_dwordx4 %0, %4" KV_NT "\n\tglobal_load_lds_dwordx4 %1, %4 offset:1024" KV_NT
               "\n\tglobal_load_lds_dwordx4 %2, %4 offset:2048" KV_NT "\n\tglobal_load_lds_dwordx4 %3, %4 offset:3072" KV_NT
               :: "v"(v0), "v"(v1), "v"(v2), "v"(v3), "s"(sbase), "s"(lds) : "memory");
}
__device__ __forceinline__ void dma16p(u32 voff, const char* sbase, u32 lds) {
  asm volatile("s_mov_b32 m0, %2\n\ts_nop 0\n\tglobal_load_lds_dwordx4 %0, %1 sc1" :: "v"(voff), "s"(sbase), "s"(lds) : "memory");
}
__device__ __forceinline__ void ld16s(u32x4& d, u32 voff, const char* sbase) {
  asm volatile("global_load_dwordx4 %0, %1, %2 sc1" : "=v"(d) : "v"(voff), "s"(sbase) : "memory");
}
__device__ __forceinline__ void st8s(u32 voff, u32x2 d, char* sbase) {
  asm volatile("global_store_dwordx2 %0, %1, %2 sc1" :: "v"(voff), "v"(d), "s"(sbase) : "memory");
}
#define WAITVM(n) asm volatile("s_waitcnt vmcnt(" #n ")" ::: "memory")
#define BARRIER() asm volatile("s_waitcnt lgkmcnt(0)\n\ts_barrier" ::: "memory")
#define PIN(x) asm volatile("" : "+v"(x))

template <typename T> __device__ __forceinline__ T lds_ld(const LAS char* base, u32 off) { return *(const LAS T*)(base + off); }
template <typename T> __device__ __forceinline__ void lds_st(LAS char* base, u32 off, T v) { *(LAS T*)(base + off) = v; }
__device__ __forceinline__ s16x4 trd(const LAS char* base, u32 off) {
  return __builtin_bit_cast(s16x4, __builtin_amdgcn_ds_read_tr16_b64_v4i16((LAS s16x4*)(base + off)));
}
__device__ __forceinline__ bf16x8 cat8(s16x4 a, s16x4 b) {
  s16x8 r = __builtin_shufflevector(a, b, 0, 1, 2, 3, 4, 5, 6, 7); return __builtin_bit_cast(bf16x8, r);
}
__device__ __forceinline__ f32x4 mfma(bf16x8 a, bf16x8 b, f32x4 c) { return __builtin_amdgcn_mfma_f32_16x16x32_bf16(a, b, c, 0, 0, 0); }
__device__ __forceinline__ u32 pkbf(float lo, float hi) {
  f32x2 v = {lo, hi}; return __builtin_bit_cast(u32, __builtin_convertvector(v, bf16x2));
}
__device__ __forceinline__ float rbf(float v) { return __uint_as_float(pkbf(v, v) << 16); }
template <int C> __device__ __forceinline__ float dpp(float v) {
  return __builtin_bit_cast(float, __builtin_amdgcn_mov_dpp(__builtin_bit_cast(int, v), C, 0xf, 0xf, true));
}
constexpr int ROR1 = 0x121, ROR2 = 0x122, ROR4 = 0x124, ROR8 = 0x128, QX1 = 0xB1, QX2 = 0x4E;
__device__ __forceinline__ float sum16(float v) { auto s = __builtin_amdgcn_permlane16_swap(__float_as_uint(v), __float_as_uint(v), false, false); return __uint_as_float(s[0]) + __uint_as_float(s[1]); }
__device__ __forceinline__ float sum32(float v) { auto s = __builtin_amdgcn_permlane32_swap(__float_as_uint(v), __float_as_uint(v), false, false); return __uint_as_float(s[0]) + __uint_as_float(s[1]); }
__device__ __forceinline__ float max16(float v) { auto s = __builtin_amdgcn_permlane16_swap(__float_as_uint(v), __float_as_uint(v), false, false); return fmaxf(__uint_as_float(s[0]), __uint_as_float(s[1])); }
__device__ __forceinline__ float max32(float v) { auto s = __builtin_amdgcn_permlane32_swap(__float_as_uint(v), __float_as_uint(v), false, false); return fmaxf(__uint_as_float(s[0]), __uint_as_float(s[1])); }
__device__ __forceinline__ float ex2(float v) { return __builtin_amdgcn_exp2f(v); }
constexpr float LOG2E = 1.4426950408889634f;
constexpr u32 SPIN_CAP = 1u << 16;

#define GCHECK "v_xor_b32 %[t0], %[T8], %[q0]\n\tv_xor_b32 %[t1], %[T8], %[q2]\n\tv_or_b32 %[t0], %[t0], %[t1]\n\tv_and_b32 %[t0], 0xff, %[t0]\n\t" \
               "v_xor_b32 %[t1], %[T32], %[q1]\n\tv_or_b32 %[t0], %[t0], %[t1]\n\tv_xor_b32 %[t1], %[T32], %[q3]\n\tv_or_b32 %[t0], %[t0], %[t1]\n\t" \
               "v_cmp_ne_u32 vcc, 0, %[t0]\n\t"

__global__ void __launch_bounds__(512, 2) fused_decode(Params unused_) {
  Params p;
  {
    u32x16 A; u32x8 B;
    asm volatile("s_load_dwordx16 %0, %2, 0x0\n\ts_load_dwordx8 %1, %2, 0x40\n\ts_waitcnt lgkmcnt(0)" : "=&s"(A), "=&s"(B) : "s"(__builtin_amdgcn_kernarg_segment_ptr()));
    p.x = mkptr<const char*>(A[0], A[1]); p.kc = mkptr<const char*>(A[2], A[3]); p.vc = mkptr<const char*>(A[4], A[5]); p.wq = mkptr<const char*>(A[6], A[7]);
    p.wo = mkptr<const char*>(A[8], A[9]); p.wg = mkptr<const char*>(A[10], A[11]); p.wu = mkptr<const char*>(A[12], A[13]); p.wd = mkptr<const char*>(A[14], A[15]);
    p.gw = mkptr<const char*>(B[0], B[1]); p.out = mkptr<char*>(B[2], B[3]); p.ws = mkptr<char*>(B[4], B[5]);
  }
  __shared__ __attribute__((aligned(1024))) char smem_[L_END];
  LAS char* sm = (LAS char*)smem_;
  const u32 tid = threadIdx.x, lane = tid & 63;
  const u32 w = __builtin_amdgcn_readfirstlane(tid >> 6);
  const u32 bid = blockIdx.x;
  const u32 h = bid >> 3, sub = (bid + h) & 7, b = sub >> 2, s = sub & 3;
  const bool roleG = bid < 172, roleD = bid >= 170;
  const u32 g = lane >> 4, col = lane & 15, r4 = (lane >> 2) & 3, c4 = lane & 3;
  const u32 hf = (lane & 1) << 4, un = (lane & 15) >> 1;
  {
    const u32 vo = g * 8192 + ((un ^ (g | ((w & 1) << 2))) << 5) + hf;
    const char* base = p.wq + (size_t)(64 * sub + 8 * w) * 8192 + h * 256;
    dma16(vo, base, L_WQ + w * 2048);
    dma16(vo + 4 * 8192, base, L_WQ + w * 2048 + 1024);
  }
  if (w < 4) dma16p(lane * 16, w == 0 ? p.x : w == 1 ? p.x + 1024 : w == 2 ? p.gw : p.ws + WS_EPOCH, L_XS + w * 1024);
  const u32 vo128 = ((((lane & 7) >> 1) ^ ((lane >> 4) & 1)) << 5) + hf;
  if (roleG) {
    const u32 ell = bid >> 2, rg = bid & 3;
    const u32 vo = (lane >> 3) * 5504 + (vo128 ^ ((w & 1) << 6));
    const u32 o = (64 * rg + 8 * w) * 5504 + ell * 128;
    dma16(vo, p.wg + o, L_GU + w * 1024);
    dma16(vo, p.wu + o, L_GU + 8192 + w * 1024);
  }
  const size_t kvrow = ((size_t)(b * 32 + h) * 4096 + 3072 + 256 * s) * 256;
  const u32 vok = g * 256 + ((col ^ g) << 4);
  const char* kbase = p.kc + kvrow + (size_t)w * 8192;
  dma4kv(vok, vok ^ 64, vok ^ 128, vok ^ 192, kbase, L_K + w * 8192);
  lds_st<u32x2>(sm, L_PM + w * 528 + lane * 8, u32x2{0u, 0u});
  if (roleG) WAITVM(6); else WAITVM(4);
  BARRIER();
  const u32 T32 = __builtin_amdgcn_readfirstlane(lds_ld<u32>(sm, L_EP)) + 1u;
  const u32 T8 = (T32 ^ 0x5Au) & 0xFFu;
  {
    const u32 xa = L_XS + (col & 1) * 1024 + (64 * sub + 8 * g) * 2;
    const u32 tb = L_WQ + (8 * g + r4) * 256 + ((w ^ (r4 | ((g & 1) << 2))) << 5) + c4 * 8;
    f32x4 acc = {0.f, 0.f, 0.f, 0.f};
#pragma unroll
    for (int t = 0; t < 2; ++t) {
      const bf16x8 a = lds_ld<bf16x8>(sm, xa + t * 64);
      const bf16x8 bb = cat8(trd(sm, tb + t * 8192), trd(sm, tb + t * 8192 + 1024));
      acc = mfma(a, bb, acc);
    }
    const float v = (g & 1) ? acc[1] : acc[0];
    if (g < 2) {
      u32x2 d = {(__float_as_uint(v) & 0xFFFFFF00u) | T8, T32};
      st8s((g * 128 + 16 * w + col) * 8, d, p.ws + WS_SQ + (size_t)(h * 8 + sub) * 2048);
    }
  }
  if (roleG) {
    WAITVM(5);
    BARRIER();
    const u32 ell = bid >> 2, rg = bid & 3;
    const u32 hl = 2 * ell + ((w & 3) >> 1);
    const u32 sel = hl >= 43 ? 0x07060302u : 0x05040100u;
    const u32 xa = L_XS + (col & 1) * 1024 + (128 * rg + 16 * g) * 2;
    const u32 tb = L_GU + (w >> 2) * 8192 + (8 * g + r4) * 128 + (((w & 3) ^ ((r4 >> 1) | ((g & 1) << 1))) << 5) + c4 * 8;
    f32x4 acc = {0.f, 0.f, 0.f, 0.f};
#pragma unroll
    for (int t = 0; t < 2; ++t) {
      const u32x4 x0 = lds_ld<u32x4>(sm, xa + t * 128), x1 = lds_ld<u32x4>(sm, xa + t * 128 + 16);
      const u32x4 a = {__builtin_amdgcn_perm(x0[1], x0[0], sel), __builtin_amdgcn_perm(x0[3], x0[2], sel),
                       __builtin_amdgcn_perm(x1[1], x1[0], sel), __builtin_amdgcn_perm(x1[3], x1[2], sel)};
      const bf16x8 bb = cat8(trd(sm, tb + t * 4096), trd(sm, tb + t * 4096 + 512));
      acc = mfma(__builtin_bit_cast(bf16x8, a), bb, acc);
    }
    const float v = (g & 1) ? acc[1] : acc[0];
    if (g < 2) {
      u32x2 d = {(__float_as_uint(v) & 0xFFFFFF00u) | T8, T32};
      st8s((((w >> 2) * 2 + g) * 32 + 16 * (w & 1) + col) * 8, d, p.ws + WS_SG + (size_t)(hl * 4 + rg) * 1024);
    }
  }
  dma4kv(vok, vok ^ 64, vok ^ 128, vok ^ 192, kbase + 4096, L_K + w * 8192 + 4096);
  const char* qsrc = p.ws + WS_SQ + (size_t)(h * 8 + w) * 2048 + b * 1024;
  asm volatile("global_load_dwordx4 v[104:107], %0, %1 sc1" :: "v"(lane * 16), "s"(qsrc) : "memory", "v104", "v105", "v106", "v107");
  const u32 vov = g * 256 + ((((lane & 15) >> 1) ^ (g | (((w >> 1) & 1) << 2))) << 5) + ((lane & 1) << 4);
  const char* vbase = p.vc + kvrow + (size_t)w * 1024;
#pragma unroll
  for (int i = 0; i < 4; ++i) dma16kv(vov, vbase + i * 8192, L_V + w * 1024 + i * 8192);
  float gate = 0.f;
  const bool fin = bid < 128 && w == 4;
  if (fin) {
    const u32x4 xv = lds_ld<u32x4>(sm, L_XS + (bid >> 6) * 1024 + lane * 16), gv = lds_ld<u32x4>(sm, L_GW + lane * 16);
    float a = 0.f;
#pragma unroll
    for (int k = 0; k < 4; ++k) {
      a += rbf(__uint_as_float(xv[k] << 16) * __uint_as_float(gv[k] << 16));
      a += rbf(__uint_as_float(xv[k] & 0xFFFF0000u) * __uint_as_float(gv[k] & 0xFFFF0000u));
    }
    a += dpp<ROR8>(a); a += dpp<ROR4>(a); a += dpp<ROR2>(a); a += dpp<ROR1>(a);
    a = sum32(sum16(a));
    a = rbf(a);
    gate = rbf(1.f / (1.f + __expf(-a)));
  }
  PIN(gate);
  {
    u32 q0, q1, q2, q3, t0, t1, cnt;
    asm volatile(
        "s_waitcnt vmcnt(4)\n\t"
        "s_mov_b32 %[cnt], 0\n"
        "1:\n\t"
        "v_mov_b32 %[q0], v104\n\tv_mov_b32 %[q1], v105\n\tv_mov_b32 %[q2], v106\n\tv_mov_b32 %[q3], v107\n\t"
        GCHECK
        "s_cbranch_vccz 9f\n\t"
        "s_add_u32 %[cnt], %[cnt], 1\n\ts_cmp_gt_u32 %[cnt], 0x10000\n\ts_cbranch_scc1 9f\n\t"
        "global_load_dwordx4 v[104:107], %[off], %[src] sc1\n\t"
        "s_waitcnt vmcnt(0)\n\t"
        "s_branch 1b\n"
        "9:\n\t"
        : [q0] "=&v"(q0), [q1] "=&v"(q1), [q2] "=&v"(q2), [q3] "=&v"(q3), [t0] "=&v"(t0), [t1] "=&v"(t1), [cnt] "=&s"(cnt)
        : [off] "v"(lane * 16), [src] "s"(qsrc), [T8] "s"(T8), [T32] "s"(T32)
        : "memory", "scc", "vcc", "v104", "v105", "v106", "v107");
    lds_st<u32x2>(sm, L_ST + w * 512 + lane * 8, u32x2{q0 & 0xFFFFFF00u, q2 & 0xFFFFFF00u});
  }
  BARRIER();
#pragma unroll
  for (int i = 4; i < 8; ++i) dma16kv(vov, vbase + i * 8192, L_V + w * 1024 + i * 8192);
  {
    f32x2 q2 = lds_ld<f32x2>(sm, L_ST + lane * 8);
#pragma unroll
    for (int j = 1; j < 8; ++j) q2 += lds_ld<f32x2>(sm, L_ST + j * 512 + lane * 8);
    lds_st<u32>(sm, L_QW + w * 256 + lane * 4, pkbf(q2[0], q2[1]));
    asm volatile("s_waitcnt lgkmcnt(0)" ::: "memory");
    const u32 kb = L_K + (32 * w + col) * 256 + ((g ^ col) << 4);
    f32x4 a0 = {0.f, 0.f, 0.f, 0.f}, a1 = a0;
#pragma unroll
    for (int t = 0; t < 4; ++t) {
      const bf16x8 aq = lds_ld<bf16x8>(sm, L_QW + w * 256 + t * 64 + g * 16);
      a0 = mfma(aq, lds_ld<bf16x8>(sm, kb ^ (t << 6)), a0);
      a1 = mfma(aq, lds_ld<bf16x8>(sm, (kb ^ (t << 6)) + 4096), a1);
    }
    const float s0 = (g & 2) ? a1[0] : a0[0];
    const float s2 = rbf(rbf(s0) * 0.08838834764831845f);
    float m = s2;
    m = fmaxf(m, dpp<ROR8>(m)); m = fmaxf(m, dpp<ROR4>(m)); m = fmaxf(m, dpp<ROR2>(m)); m = fmaxf(m, dpp<ROR1>(m));
    m = max32(m);
    const float e = ex2((s2 - m) * LOG2E);
    float l = e;
    l += dpp<ROR8>(l); l += dpp<ROR4>(l); l += dpp<ROR2>(l); l += dpp<ROR1>(l);
    l = sum32(l);
    if ((g & 1) == 0) lds_st<unsigned short>(sm, L_PM + w * 528 + (32 * w + 16 * (g >> 1) + col) * 2, (unsigned short)pkbf(e, e));
    if (lane < 2) lds_st<float>(sm, L_SM + lane * 32 + w * 4, lane ? l : m);
  }
  BARRIER();
  {
    const u32 vo = (lane >> 3) * 1024 + vo128;
    const char* base = p.wo + (size_t)(h * 128 + 16 * w) * 1024 + sub * 128;
    dma16(vo, base, L_WO + (16 * w) * 128);
    dma16(vo ^ 64, base + 8 * 1024, L_WO + (16 * w + 8) * 128);
  }
  const u32 rd = (bid - 170) >> 1, cd = (bid - 170) & 1;
  const char* gsrc = p.ws + WS_SG + (size_t)(((w >> 2) * 43 + rd) * 4 + (w & 3)) * 1024;
  if (roleD) {
    asm volatile("global_load_dwordx4 v[108:111], %0, %1 sc1" :: "v"(lane * 16), "s"(gsrc) : "memory", "v108", "v109", "v110", "v111");
    const u32 f0 = (lane >> 5) | (((w >> 1) & 1) << 2);
    const u32 vo = (lane >> 5) * 1024 + ((((lane & 31) >> 1) ^ f0) << 5) + ((lane & 1) << 4);
    const char* base = p.wd + (size_t)(32 * rd + 4 * w) * 1024 + cd * 512;
    dma16(vo, base, L_WD + (4 * w) * 512);
    dma16(vo ^ 64, base + 2 * 1024, L_WD + (4 * w + 2) * 512);
  }
  float cw, Lw, Mw;
  {
    const u32 j4 = (((g & 1) << 2) | c4) * 4;
    const float mj = lds_ld<float>(sm, L_SM + j4), lj = lds_ld<float>(sm, L_SM + 32 + j4);
    float M = fmaxf(mj, dpp<QX1>(mj)); M = fmaxf(M, dpp<QX2>(M));
    Mw = max16(M);
    cw = ex2((mj - Mw) * LOG2E);
    float lp = cw * lj; lp += dpp<QX1>(lp); lp += dpp<QX2>(lp);
    Lw = sum16(lp);
  }
  bf16x8 ap[8];
#pragma unroll
  for (int t = 0; t < 8; ++t) ap[t] = lds_ld<bf16x8>(sm, L_PM + (col & 7) * 528 + t * 64 + g * 16);
  const u32 vb = L_V + (8 * g + r4) * 256 + ((w ^ (r4 | ((g & 1) << 2))) << 5) + c4 * 8;
  u32 ooff = lane < 16 ? (8 * w + (col >> 1)) * 8 : 512 + (lane - 16) * 8;
  u32 ostat = (__float_as_uint(lane == 16 ? Mw : Lw) & 0xFFFFFF00u) | T8;
  const u64 omask = 0x5555ull | (w == 0 ? 0x30000ull : 0ull);
  char* odst = p.ws + WS_SO + (size_t)(h * 8 + sub) * 640;
  float cw0 = dpp<0x00>(cw), cw1 = dpp<0x55>(cw), cw2 = dpp<0xAA>(cw), cw3 = dpp<0xFF>(cw);
  PIN(ooff); PIN(ostat); PIN(cw0); PIN(cw1); PIN(cw2); PIN(cw3);
  f32x4 oa = {0.f, 0.f, 0.f, 0.f};
  if (roleD) WAITVM(7); else WAITVM(4);
  BARRIER();
#pragma unroll
  for (int t = 0; t < 6; ++t) oa = mfma(ap[t], cat8(trd(sm, vb + t * 8192), trd(sm, vb + t * 8192 + 1024)), oa);
  PIN(oa);
  if (roleD) WAITVM(5); else WAITVM(2);
  BARRIER();
#pragma unroll
  for (int t = 6; t < 8; ++t) oa = mfma(ap[t], cat8(trd(sm, vb + t * 8192), trd(sm, vb + t * 8192 + 1024)), oa);
  {
    float v = oa[0] * cw0;
    v = fmaf(oa[1], cw1, v); v = fmaf(oa[2], cw2, v); v = fmaf(oa[3], cw3, v);
    v = sum16(v);
    const u32 pr = pkbf(v, dpp<QX1>(v));
    u32x2 d = {lane < 16 ? pr : ostat, T32};
    asm volatile("s_mov_b64 exec, %3\n\tglobal_store_dwordx2 %0, %1, %2 sc1\n\ts_mov_b64 exec, -1" :: "v"(ooff), "v"(d), "s"(odst), "s"(omask) : "memory");
  }
  WAITVM(1);
  if (roleD) {
    u32 q0, q1, q2, q3, t0, t1, cnt;
    asm volatile(
        "s_mov_b32 %[cnt], 0\n"
        "1:\n\t"
        "v_mov_b32 %[q0], v108\n\tv_mov_b32 %[q1], v109\n\tv_mov_b32 %[q2], v110\n\tv_mov_b32 %[q3], v111\n\t"
        GCHECK
        "s_cbranch_vccz 9f\n\t"
        "s_add_u32 %[cnt], %[cnt], 1\n\ts_cmp_gt_u32 %[cnt], 0x10000\n\ts_cbranch_scc1 9f\n\t"
        "global_load_dwordx4 v[108:111], %[off], %[src] sc1\n\t"
        "s_waitcnt vmcnt(0)\n\t"
        "s_branch 1b\n"
        "9:\n\t"
        : [q0] "=&v"(q0), [q1] "=&v"(q1), [q2] "=&v"(q2), [q3] "=&v"(q3), [t0] "=&v"(t0), [t1] "=&v"(t1), [cnt] "=&s"(cnt)
        : [off] "v"(lane * 16), [src] "s"(gsrc), [T8] "s"(T8), [T32] "s"(T32)
        : "memory", "scc", "vcc", "v108", "v109", "v110", "v111");
    lds_st<u32x2>(sm, L_ST + w * 512 + lane * 8, u32x2{q0 & 0xFFFFFF00u, q2 & 0xFFFFFF00u});
  }
  u32 wdt = L_WD + (8 * g + r4) * 512 + (((4 * (w & 3)) ^ (r4 | ((g & 1) << 2))) << 5) + c4 * 8;
  u32 hwr = L_QW + w * 256 + lane * 4, hrd = L_QW + w * 256 + (col & 1) * 64 + g * 16;
  u32 boff = ((g & 1) * 512 + 256 * cd + 16 * (4 * (w & 3) + (g >> 1)) + col) * 8;
  char* bdst = p.ws + WS_SB + (size_t)rd * 8192;
  PIN(wdt); PIN(hwr); PIN(hrd); PIN(boff);
  BARRIER();
  if (w < 4) __builtin_amdgcn_s_setprio(2);
  const char* osrc = p.ws + WS_SO + (size_t)(h * 8) * 640;
  const u32 soff = (((g & 1) << 2) | c4) * 640 + 512;
  bf16x8 wb[4];
  if (__builtin_expect(roleD && w >= 4, 0)) {
    bf16x8 wf[4];
#pragma unroll
    for (int q = 0; q < 4; ++q) wf[q] = cat8(trd(sm, wdt ^ (q << 5)), trd(sm, (wdt ^ (q << 5)) + 2048));
    f32x2 gs[8];
#pragma unroll
    for (int j = 0; j < 8; ++j) gs[j] = lds_ld<f32x2>(sm, L_ST + j * 512 + lane * 8);
    __builtin_amdgcn_sched_barrier(0);
    f32x2 v2 = gs[0];
#pragma unroll
    for (int j = 1; j < 8; ++j) v2 += gs[j];
    const u32 pr = pkbf(v2[0], v2[1]);
    auto sw = __builtin_amdgcn_permlane32_swap(pr, pr, false, false);
    const u32 gq = sw[0], uq = sw[1];
    const u32 hp = pkbf(__uint_as_float(gq << 16) * __uint_as_float(uq << 16), __uint_as_float(gq & 0xFFFF0000u) * __uint_as_float(uq & 0xFFFF0000u));
    lds_st<u32>(sm, hwr, hp);
    asm volatile("s_waitcnt lgkmcnt(0)" ::: "memory");
    const bf16x8 ah = lds_ld<bf16x8>(sm, hrd);
    const f32x4 z = {0.f, 0.f, 0.f, 0.f};
    f32x4 acc[4];
#pragma unroll
    for (int q = 0; q < 4; ++q) acc[q] = mfma(ah, wf[q], z);
    asm volatile("" : "+v"(acc[0]), "+v"(acc[1]), "+v"(acc[2]), "+v"(acc[3]) : "v"(wf[0]), "v"(wf[1]), "v"(wf[2]), "v"(wf[3]));
#pragma unroll
    for (int qq = 0; qq < 2; ++qq) {
      const f32x4 x0 = acc[2 * qq], x1 = acc[2 * qq + 1];
      const float lo = (g & 1) ? x0[1] : x0[0], hi = (g & 1) ? x1[1] : x1[0];
      const float v = (g & 2) ? hi : lo;
      u32x2 d = {(__float_as_uint(v) & 0xFFFFFF00u) | T8, T32};
      st8s(boff + qq * 256, d, bdst);
    }
  }
  if (__builtin_expect(w < 4, 1)) {
    const u32 tb = L_WO + (8 * g + r4) * 128 + ((w ^ ((r4 >> 1) | ((g & 1) << 1))) << 5) + c4 * 8;
#pragma unroll
    for (int t = 0; t < 4; ++t) wb[t] = cat8(trd(sm, tb + t * 4096), trd(sm, tb + t * 4096 + 512));
  }
  u32 foff = ((g & 1) * 512 + 64 * sub + 16 * w + col) * 8, aoff = L_OS + (col & 7) * 272 + g * 16;
  char* fdst = p.ws + WS_SF + (size_t)h * 8192;
  PIN(foff); PIN(aoff);
  u32 sM, sL;
  {
    u32 t0, t1, cnt;
    u32 ooffr = w * 640 + lane * 8, osta = L_OS + w * 272 + lane * 4;
    asm volatile(
        "s_mov_b32 %[cnt], 0\n"
        "1:\n\t"
        "global_load_dwordx2 v[104:105], %[oo], %[src] sc1\n\t"
        "global_load_dwordx4 v[108:111], %[so], %[src] sc1\n\t"
        "s_waitcnt vmcnt(0)\n\t"
        "v_xor_b32 %[t0], %[T8], v108\n\tv_xor_b32 %[t1], %[T8], v110\n\tv_or_b32 %[t0], %[t0], %[t1]\n\tv_and_b32 %[t0], 0xff, %[t0]\n\t"
        "v_xor_b32 %[t1], %[T32], v105\n\tv_or_b32 %[t0], %[t0], %[t1]\n\tv_xor_b32 %[t1], %[T32], v109\n\tv_or_b32 %[t0], %[t0], %[t1]\n\t"
        "v_xor_b32 %[t1], %[T32], v111\n\tv_or_b32 %[t0], %[t0], %[t1]\n\t"
        "v_cmp_ne_u32 vcc, 0, %[t0]\n\ts_cbranch_vccz 9f\n\t"
        "s_add_u32 %[cnt], %[cnt], 1\n\ts_cmp_lt_u32 %[cnt], 0x10000\n\ts_cbranch_scc1 1b\n"
        "9:\n\t"
        "ds_write_b32 %[osta], v104\n\t"
        "v_and_b32 %[sM], 0xffffff00, v108\n\tv_and_b32 %[sL], 0xffffff00, v110\n\ts_nop 1\n\t"
        : [sM] "=&v"(sM), [sL] "=&v"(sL), [t0] "=&v"(t0), [t1] "=&v"(t1), [cnt] "=&s"(cnt)
        : [oo] "v"(ooffr), [so] "v"(soff), [src] "s"(osrc), [osta] "v"(osta), [T8] "s"(T8), [T32] "s"(T32)
        : "memory", "scc", "vcc", "v104", "v105", "v108", "v109", "v110", "v111");
  }
  BARRIER();
  if (__builtin_expect(w >= 4, 0)) {
    if (fin) {
      const u32 fb = bid >> 6, n0 = (bid & 63) * 8;
      const u32 hh = lane >> 2;
      const u32 o0 = (fb * 512 + n0 + 2 * c4) * 8 + hh * 8192;
      const u32 hh2 = hh < 11 ? hh : 0u;
      const u32 o2 = (fb * 512 + n0 + 2 * c4) * 8 + (32 + hh2) * 8192;
      const char* fa = p.ws + WS_SF; const char* fbp = p.ws + WS_SB;
      u32 outoff = (fb * 512 + n0 + 2 * lane) * 2;
      PIN(outoff);
      u32x4 A0, A1, B0, B1, B2;
      for (u32 spin = 0;; ++spin) {
        ld16s(B0, o0, fbp); ld16s(B1, o0, fbp + 16 * 8192); ld16s(B2, o2, fbp);
        asm volatile("s_waitcnt vmcnt(0)" : "+v"(B0), "+v"(B1), "+v"(B2) :: "memory");
        const u32 lo = (B0[0] ^ T8) | (B0[2] ^ T8) | (B1[0] ^ T8) | (B1[2] ^ T8) | (B2[0] ^ T8) | (B2[2] ^ T8);
        const u32 hi = (B0[1] ^ T32) | (B0[3] ^ T32) | (B1[1] ^ T32) | (B1[3] ^ T32) | (B2[1] ^ T32) | (B2[3] ^ T32);
        if (__builtin_amdgcn_ballot_w64(((lo & 0xFFu) | hi) != 0) == 0 || spin > SPIN_CAP) break;
      }
      auto val = [](u32 v) { return __uint_as_float(v & 0xFFFFFF00u); };
      float b0 = val(B0[0]) + val(B1[0]), b1 = val(B0[2]) + val(B1[2]);
      if (hh < 11) { b0 += val(B2[0]); b1 += val(B2[2]); }
      b0 += dpp<ROR8>(b0); b1 += dpp<ROR8>(b1);
      b0 += dpp<ROR4>(b0); b1 += dpp<ROR4>(b1);
      b0 = sum32(sum16(b0)); b1 = sum32(sum16(b1));
      const float gm = rbf(1.f - gate);
      float t0 = rbf(gm * rbf(b0)), t1 = rbf(gm * rbf(b1));
      PIN(t0); PIN(t1);
      for (u32 spin = 0;; ++spin) {
        ld16s(A0, o0, fa); ld16s(A1, o0, fa + 16 * 8192);
        asm volatile("s_waitcnt vmcnt(0)" : "+v"(A0), "+v"(A1) :: "memory");
        const u32 lo = (A0[0] ^ T8) | (A0[2] ^ T8) | (A1[0] ^ T8) | (A1[2] ^ T8);
        const u32 hi = (A0[1] ^ T32) | (A0[3] ^ T32) | (A1[1] ^ T32) | (A1[3] ^ T32);
        if (__builtin_amdgcn_ballot_w64(((lo & 0xFFu) | hi) != 0) == 0 || spin > SPIN_CAP) break;
      }
      float a0 = val(A0[0]) + val(A1[0]), a1 = val(A0[2]) + val(A1[2]);
      a0 += dpp<ROR8>(a0); a1 += dpp<ROR8>(a1);
      a0 += dpp<ROR4>(a0); a1 += dpp<ROR4>(a1);
      a0 = sum32(sum16(a0)); a1 = sum32(sum16(a1));
      const float r0 = rbf(gate * rbf(a0)) + t0, r1 = rbf(gate * rbf(a1)) + t1;
      asm volatile("s_mov_b64 exec, 15\n\tglobal_store_dword %0, %1, %2\n\ts_mov_b64 exec, -1" :: "v"(outoff), "v"(pkbf(r0, r1)), "s"(p.out) : "memory");
      if (bid == 0) asm volatile("s_mov_b64 exec, 1\n\tglobal_store_dword %0, %1, %2 sc1\n\ts_mov_b64 exec, -1" :: "v"(0u), "v"(T32), "s"(p.ws) : "memory");
    }
    return;
  }
  bf16x8 a0 = lds_ld<bf16x8>(sm, aoff), a1 = lds_ld<bf16x8>(sm, aoff + 64), a2 = lds_ld<bf16x8>(sm, aoff + 128), a3 = lds_ld<bf16x8>(sm, aoff + 192);
  __builtin_amdgcn_sched_barrier(0);
  float c;
  {
    const float Mj = __uint_as_float(sM), Lj = __uint_as_float(sL);
    float Mb = fmaxf(Mj, dpp<QX1>(Mj)); Mb = fmaxf(Mb, dpp<QX2>(Mb));
    const float e = ex2((Mj - Mb) * LOG2E);
    float Lb = e * Lj; Lb += dpp<QX1>(Lb); Lb += dpp<QX2>(Lb);
    c = e * __builtin_amdgcn_rcpf(Lb);
  }
  float c0 = dpp<0x00>(c), c1 = dpp<0x55>(c), c2 = dpp<0xAA>(c), c3 = dpp<0xFF>(c);

  asm volatile("" : "+v"(a0), "+v"(a1), "+v"(a2), "+v"(a3), "+v"(c0), "+v"(c1), "+v"(c2), "+v"(c3));
  const f32x4 z = {0.f, 0.f, 0.f, 0.f};
  f32x4 acc = mfma(a0, wb[0], z), acc2 = mfma(a2, wb[2], z);
  acc = mfma(a1, wb[1], acc); acc2 = mfma(a3, wb[3], acc2);
  acc += acc2;
  float v = acc[0] * c0;
  v = fmaf(acc[1], c1, v); v = fmaf(acc[2], c2, v); v = fmaf(acc[3], c3, v);
  const u32x2 d = {(__float_as_uint(v) & 0xFFFFFF00u) | T8, T32};
  asm volatile("s_mov_b64 exec, %3\n\tglobal_store_dwordx2 %0, %1, %2 sc1\n\ts_mov_b64 exec, -1" :: "v"(foff), "v"(d), "s"(fdst), "s"(0xFFFFFFFFull) : "memory");
}

extern "C" void kernel_launch(void* const* d_in, const int* in_sizes, int n_in, void* d_out, int out_size, void* d_ws, size_t ws_size, hipStream_t stream) {
  Params p;
  p.x = (const char*)d_in[0]; p.kc = (const char*)d_in[1]; p.vc = (const char*)d_in[2]; p.wq = (const char*)d_in[3]; p.wo = (const char*)d_in[4];
  p.wg = (const char*)d_in[6]; p.wu = (const char*)d_in[7]; p.wd = (const char*)d_in[8]; p.gw = (const char*)d_in[9];
  p.out = (char*)d_out; p.ws = (char*)d_ws; p.unused = 0;
  fused_decode<<<256, 512, 0, stream>>>(p);
}
```
